```python
import torch

def _softmax(x, axis=-1):
    return torch.softmax(x, dim=axis)
def _gelu(x):
    return torch.nn.functional.gelu(x, approximate="tanh")
def _silu(x):
    return torch.nn.functional.silu(x)
def _sigmoid(x):
    return torch.sigmoid(x)
def _layernorm(x, axis=-1, eps=1e-5):
    m = x.mean(dim=axis, keepdim=True)
    v = x.var(dim=axis, keepdim=True, unbiased=False)
    return (x - m) / torch.sqrt(v + eps)
def _scatter_add(tgt, idx, src, axis=0):
    return tgt.clone().index_add_(axis, idx, src)
def _segment_sum(src, seg, n):
    out = torch.zeros((n,) + src.shape[1:], dtype=src.dtype)
    return out.index_add_(0, seg, src)
def _pad(x, pw):
    flat = tuple(v for pair in reversed(pw) for v in pair)
    return torch.nn.functional.pad(x, flat)

kernel_name = 'sequential_preLN__sliding_block_cached__mixer_channel_mlp__decode'

def setup_inputs():

    _w = (lambda a: (lambda t: t.to(torch.bfloat16) if t.is_floating_point() else t)(torch.as_tensor(a)))
    def _A_setup():
        B, Sq, D = 8, 1, 512
        Hq, Hkv, Sk, Dh = 32, 8, 4096, 128
        assert Hq % Hkv == 0, (Hq, Hkv)
        return {
            "x": _w((lambda *s: torch.randn(*s, dtype=torch.bfloat16) * 0.05)(B, Sq, D)),
            "k_cache": _w((lambda *s: torch.randn(*s, dtype=torch.bfloat16) * 3.2)(B, Hkv, Sk, Dh)),
            "v_cache": _w((lambda *s: torch.randn(*s, dtype=torch.bfloat16) * 0.4)(B, Hkv, Sk, Dh)),
            "wq": _w((lambda *s: torch.randn(*s, dtype=torch.bfloat16) * 3.2)(D, Hq * Dh) * (D ** -0.5)),
            "wo": _w((lambda *s: torch.randn(*s, dtype=torch.bfloat16) * 0.4)(Hq * Dh, D) * ((Hq * Dh) ** -0.5)),
        }
    _A_in = _A_setup()

    def _B_setup():
        B, S, D, F = 8, 1, 512, 1792
        return {
            "x": _w((lambda *s: torch.randn(*s, dtype=torch.bfloat16) * 0.05)(B, S, D)),
            "w1": _w((lambda *s: torch.randn(*s, dtype=torch.bfloat16) * 3.2)(D, F) * (D ** -0.5)),
            "b1": _w((lambda *s: torch.randn(*s, dtype=torch.bfloat16) * 1.6)(F)),
            "w2": _w((lambda *s: torch.randn(*s, dtype=torch.bfloat16) * 0.2)(F, D) * (F ** -0.5)),
            "b2": _w((lambda *s: torch.randn(*s, dtype=torch.bfloat16) * 0.4)(D)),
        }
    _B_in = _B_setup()

    return {
        "A_x": _A_in["x"],
        "A_k_cache": _A_in["k_cache"],
        "A_v_cache": _A_in["v_cache"],
        "A_wq": _A_in["wq"],
        "A_wo": _A_in["wo"],
        "B_x": _B_in["x"],
        "B_w1": _B_in["w1"],
        "B_b1": _B_in["b1"],
        "B_w2": _B_in["w2"],
        "B_b2": _B_in["b2"],
    }

def reference(A_x, A_k_cache, A_v_cache, A_wq, A_wo, B_x, B_w1, B_b1, B_w2, B_b2):
    def _A_ref(x, k_cache, v_cache, wq, wo):
        B, Sq, D = x.shape
        Hq, Dh = 32, 128
        Hkv, Sk = k_cache.shape[1], k_cache.shape[2]
        W = min(4096, max(256, Sk // 4))
        rep = Hq // Hkv
        q = (lambda x, *d: x.permute(*d))((x @ wq).reshape(B, Sq, Hq, Dh), 0, 2, 1, 3)
        k = (lambda x, n, axis: torch.repeat_interleave(x, n, dim=axis))(k_cache[:, :, Sk - W:, :], rep, axis=1)
        v = (lambda x, n, axis: torch.repeat_interleave(x, n, dim=axis))(v_cache[:, :, Sk - W:, :], rep, axis=1)
        scores = torch.einsum("bhqd,bhkd->bhqk", q, k) * (Dh ** -0.5)
        p = _softmax(scores, axis=-1)
        o = torch.einsum("bhqk,bhkd->bhqd", p, v)
        return (lambda x, *d: x.permute(*d))(o, 0, 2, 1, 3).reshape(B, Sq, Hq * Dh) @ wo

    def _B_ref(x, w1, b1, w2, b2):
        y = _layernorm(x, axis=-1)
        h = _gelu(torch.einsum("bsd,df->bsf", y, w1) + b1)
        return torch.einsum("bsf,fd->bsd", h, w2) + b2

    x = A_x
    h = x + _A_ref(_layernorm(x, axis=-1), A_k_cache, A_v_cache, A_wq, A_wo)
    h2 = h + _B_ref(_layernorm(h, axis=-1), B_w1, B_b1, B_w2, B_b2)
    return h2
```

```cpp
#include <hip/hip_runtime.h>
#include <hip/hip_bf16.h>
#include <stdint.h>

typedef unsigned short u16;
__device__ __forceinline__ float bf2f(u16 v) { return __uint_as_float(((unsigned)v) << 16); }
__device__ __forceinline__ u16 f2bf(float f) { unsigned u = __float_as_uint(f); u += 0x7fffu + ((u >> 16) & 1u); return (u16)(u >> 16); }

constexpr int B = 8, D = 512, HQ = 32, HKV = 8, SK = 4096, DH = 128, W = 1024, F = 1792;

__device__ float block_sum(float v, float* red) {
  for (int o = 32; o > 0; o >>= 1) v += __shfl_xor(v, o);
  int w = threadIdx.x >> 6;
  __syncthreads();
  if ((threadIdx.x & 63) == 0) red[w] = v;
  __syncthreads();
  float s = 0;
  for (int i = 0; i < (int)(blockDim.x >> 6); ++i) s += red[i];
  return s;
}
__device__ float block_max(float v, float* red) {
  for (int o = 32; o > 0; o >>= 1) v = fmaxf(v, __shfl_xor(v, o));
  int w = threadIdx.x >> 6;
  __syncthreads();
  if ((threadIdx.x & 63) == 0) red[w] = v;
  __syncthreads();
  float s = -INFINITY;
  for (int i = 0; i < (int)(blockDim.x >> 6); ++i) s = fmaxf(s, red[i]);
  return s;
}

__global__ void k_q(const u16* x, const u16* wq, float* q) {
  __shared__ float xs[D]; __shared__ float red[8];
  int b = blockIdx.y, t = threadIdx.x;
  float v0 = bf2f(x[b * D + t]), v1 = bf2f(x[b * D + t + 256]);
  float m = block_sum(v0 + v1, red) / D;
  float d0 = v0 - m, d1 = v1 - m;
  float var = block_sum(d0 * d0 + d1 * d1, red) / D;
  float r = 1.0f / sqrtf(var + 1e-5f);
  xs[t] = d0 * r; xs[t + 256] = d1 * r;
  __syncthreads();
  int n = blockIdx.x * 256 + t;
  float acc = 0;
  for (int k = 0; k < D; ++k) acc += xs[k] * bf2f(wq[(size_t)k * (HQ * DH) + n]);
  q[b * HQ * DH + n] = acc;
}
__global__ void k_attn(const float* q, const u16* kc, const u16* vc, float* o) {
  __shared__ float qs[DH]; __shared__ float p[W]; __shared__ float red[8];
  int h = blockIdx.x, b = blockIdx.y, t = threadIdx.x, hk = h / 4;
  if (t < DH) qs[t] = q[b * HQ * DH + h * DH + t];
  __syncthreads();
  const u16* kb = kc + ((size_t)(b * HKV + hk) * SK + (SK - W)) * DH;
  const u16* vb = vc + ((size_t)(b * HKV + hk) * SK + (SK - W)) * DH;
  float mx = -INFINITY;
  for (int j = t; j < W; j += 256) {
    float s = 0;
    for (int d = 0; d < DH; ++d) s += qs[d] * bf2f(kb[(size_t)j * DH + d]);
    s *= 0.08838834764831845f;
    p[j] = s; mx = fmaxf(mx, s);
  }
  mx = block_max(mx, red);
  float sum = 0;
  for (int j = t; j < W; j += 256) { float e = expf(p[j] - mx); p[j] = e; sum += e; }
  sum = block_sum(sum, red);
  __syncthreads();
  if (t < DH) {
    float acc = 0;
    for (int j = 0; j < W; ++j) acc += p[j] * bf2f(vb[(size_t)j * DH + t]);
    o[b * HQ * DH + h * DH + t] = acc / sum;
  }
}
__global__ void k_wo(const u16* x, const float* o, const u16* wo, float* hh, float* y) {
  __shared__ float red[8];
  int b = blockIdx.x, t = threadIdx.x;
  float acc = 0;
  for (int k = 0; k < HQ * DH; ++k) acc += o[b * HQ * DH + k] * bf2f(wo[(size_t)k * D + t]);
  float h = bf2f(x[b * D + t]) + acc;
  hh[b * D + t] = h;
  float m = block_sum(h, red) / D; float d = h - m; float var = block_sum(d * d, red) / D;
  float z = d / sqrtf(var + 1e-5f);
  m = block_sum(z, red) / D; d = z - m; var = block_sum(d * d, red) / D;
  y[b * D + t] = d / sqrtf(var + 1e-5f);
}
__global__ void k_w1(const float* y, const u16* w1, const u16* b1, float* hid) {
  int b = blockIdx.y, f = blockIdx.x * 256 + threadIdx.x;
  float acc = 0;
  for (int k = 0; k < D; ++k) acc += y[b * D + k] * bf2f(w1[(size_t)k * F + f]);
  acc += bf2f(b1[f]);
  float g = 0.5f * acc * (1.0f + tanhf(0.7978845608028654f * (acc + 0.044715f * acc * acc * acc)));
  hid[b * F + f] = g;
}
__global__ void k_w2(const float* hid, const u16* w2, const u16* b2, const float* hh, u16* out) {
  int b = blockIdx.x, t = threadIdx.x;
  float acc = 0;
  for (int k = 0; k < F; ++k) acc += hid[b * F + k] * bf2f(w2[(size_t)k * D + t]);
  out[b * D + t] = f2bf(hh[b * D + t] + acc + bf2f(b2[t]));
}

extern "C" void kernel_launch(void* const* d_in, const int* in_sizes, int n_in, void* d_out, int out_size, void* d_ws, size_t ws_size, hipStream_t stream) {
  const u16* x = (const u16*)d_in[0]; const u16* kc = (const u16*)d_in[1]; const u16* vc = (const u16*)d_in[2];
  const u16* wq = (const u16*)d_in[3]; const u16* wo = (const u16*)d_in[4];
  const u16* w1 = (const u16*)d_in[6]; const u16* b1 = (const u16*)d_in[7]; const u16* w2 = (const u16*)d_in[8]; const u16* b2 = (const u16*)d_in[9];
  float* ws = (float*)d_ws;
  float* q = ws; float* o = q + B * HQ * DH; float* hh = o + B * HQ * DH; float* y = hh + B * D; float* hid = y + B * D;
  k_q<<<dim3(16, B), 256, 0, stream>>>(x, wq, q);
  k_attn<<<dim3(HQ, B), 256, 0, stream>>>(q, kc, vc, o);
  k_wo<<<B, 512, 0, stream>>>(x, o, wo, hh, y);
  k_w1<<<dim3(F / 256, B), 256, 0, stream>>>(y, w1, b1, hid);
  k_w2<<<B, 512, 0, stream>>>(hid, w2, b2, hh, (u16*)d_out);
}
```

```cpp
#include <hip/hip_runtime.h>
#include <hip/hip_bf16.h>
#include <stdint.h>

typedef unsigned short u16;
__device__ __forceinline__ float bf2f(u16 v) { return __uint_as_float(((unsigned)v) << 16); }
__device__ __forceinline__ u16 f2bf(float f) { unsigned u = __float_as_uint(f); u += 0x7fffu + ((u >> 16) & 1u); return (u16)(u >> 16); }

__device__ __forceinline__ float rb(float f) { return bf2f(f2bf(f)); }
constexpr int B = 8, D = 512, HQ = 32, HKV = 8, SK = 4096, DH = 128, W = 1024, F = 1792;

__device__ float block_sum(float v, float* red) {
  for (int o = 32; o > 0; o >>= 1) v += __shfl_xor(v, o);
  int w = threadIdx.x >> 6;
  __syncthreads();
  if ((threadIdx.x & 63) == 0) red[w] = v;
  __syncthreads();
  float s = 0;
  for (int i = 0; i < (int)(blockDim.x >> 6); ++i) s += red[i];
  return s;
}
__device__ float block_max(float v, float* red) {
  for (int o = 32; o > 0; o >>= 1) v = fmaxf(v, __shfl_xor(v, o));
  int w = threadIdx.x >> 6;
  __syncthreads();
  if ((threadIdx.x & 63) == 0) red[w] = v;
  __syncthreads();
  float s = -INFINITY;
  for (int i = 0; i < (int)(blockDim.x >> 6); ++i) s = fmaxf(s, red[i]);
  return s;
}

__global__ void k_q(const u16* x, const u16* wq, float* q) {
  __shared__ float xs[D]; __shared__ float red[8];
  int b = blockIdx.y, t = threadIdx.x;
  float v0 = bf2f(x[b * D + t]), v1 = bf2f(x[b * D + t + 256]);
  float m = block_sum(v0 + v1, red) / D;
  float d0 = v0 - m, d1 = v1 - m;
  float var = block_sum(d0 * d0 + d1 * d1, red) / D;
  float mb = rb(m), den = rb(sqrtf(rb(rb(var) + 1e-5f)));
  xs[t] = rb(rb(v0 - mb) / den); xs[t + 256] = rb(rb(v1 - mb) / den);
  __syncthreads();
  int n = blockIdx.x * 256 + t;
  float acc = 0;
  for (int k = 0; k < D; ++k) acc += xs[k] * bf2f(wq[(size_t)k * (HQ * DH) + n]);
  q[b * HQ * DH + n] = rb(acc);
}
__global__ void k_attn(const float* q, const u16* kc, const u16* vc, float* o) {
  __shared__ float qs[DH]; __shared__ float p[W]; __shared__ float red[8];
  int h = blockIdx.x, b = blockIdx.y, t = threadIdx.x, hk = h / 4;
  if (t < DH) qs[t] = q[b * HQ * DH + h * DH + t];
  __syncthreads();
  const u16* kb = kc + ((size_t)(b * HKV + hk) * SK + (SK - W)) * DH;
  const u16* vb = vc + ((size_t)(b * HKV + hk) * SK + (SK - W)) * DH;
  float mx = -INFINITY;
  for (int j = t; j < W; j += 256) {
    float s = 0;
    for (int d = 0; d < DH; ++d) s += qs[d] * bf2f(kb[(size_t)j * DH + d]);
    s = rb(rb(s) * 0.08838834764831845f);
    p[j] = s; mx = fmaxf(mx, s);
  }
  mx = block_max(mx, red);
  float sum = 0;
  for (int j = t; j < W; j += 256) { float e = expf(p[j] - mx); p[j] = e; sum += e; }
  sum = block_sum(sum, red);
  __syncthreads();
  if (t < DH) {
    float acc = 0; float inv = 1.0f / sum;
    for (int j = 0; j < W; ++j) acc += rb(p[j] * inv) * bf2f(vb[(size_t)j * DH + t]);
    o[b * HQ * DH + h * DH + t] = rb(acc);
  }
}
__global__ void k_wo(const u16* x, const float* o, const u16* wo, float* hh, float* y) {
  __shared__ float red[8];
  int b = blockIdx.x, t = threadIdx.x;
  float acc = 0;
  for (int k = 0; k < HQ * DH; ++k) acc += o[b * HQ * DH + k] * bf2f(wo[(size_t)k * D + t]);
  float h = rb(bf2f(x[b * D + t]) + rb(acc));
  hh[b * D + t] = h;
  float m = block_sum(h, red) / D; float d = h - m; float var = block_sum(d * d, red) / D;
  float z = rb(rb(h - rb(m)) / rb(sqrtf(rb(rb(var) + 1e-5f))));
  m = block_sum(z, red) / D; d = z - m; var = block_sum(d * d, red) / D;
  y[b * D + t] = rb(rb(z - rb(m)) / rb(sqrtf(rb(rb(var) + 1e-5f))));
}
__global__ void k_w1(const float* y, const u16* w1, const u16* b1, float* hid) {
  int b = blockIdx.y, f = blockIdx.x * 256 + threadIdx.x;
  float acc = 0;
  for (int k = 0; k < D; ++k) acc += y[b * D + k] * bf2f(w1[(size_t)k * F + f]);
  acc = rb(rb(acc) + bf2f(b1[f]));
  float g = 0.5f * acc * (1.0f + tanhf(0.7978845608028654f * (acc + 0.044715f * acc * acc * acc)));
  hid[b * F + f] = rb(g);
}
__global__ void k_w2(const float* hid, const u16* w2, const u16* b2, const float* hh, u16* out) {
  int b = blockIdx.x, t = threadIdx.x;
  float acc = 0;
  for (int k = 0; k < F; ++k) acc += hid[b * F + k] * bf2f(w2[(size_t)k * D + t]);
  out[b * D + t] = f2bf(hh[b * D + t] + rb(rb(acc) + bf2f(b2[t])));
}

extern "C" void kernel_launch(void* const* d_in, const int* in_sizes, int n_in, void* d_out, int out_size, void* d_ws, size_t ws_size, hipStream_t stream) {
  const u16* x = (const u16*)d_in[0]; const u16* kc = (const u16*)d_in[1]; const u16* vc = (const u16*)d_in[2];
  const u16* wq = (const u16*)d_in[3]; const u16* wo = (const u16*)d_in[4];
  const u16* w1 = (const u16*)d_in[6]; const u16* b1 = (const u16*)d_in[7]; const u16* w2 = (const u16*)d_in[8]; const u16* b2 = (const u16*)d_in[9];
  float* ws = (float*)d_ws;
  float* q = ws; float* o = q + B * HQ * DH; float* hh = o + B * HQ * DH; float* y = hh + B * D; float* hid = y + B * D;
  k_q<<<dim3(16, B), 256, 0, stream>>>(x, wq, q);
  k_attn<<<dim3(HQ, B), 256, 0, stream>>>(q, kc, vc, o);
  k_wo<<<B, 512, 0, stream>>>(x, o, wo, hh, y);
  k_w1<<<dim3(F / 256, B), 256, 0, stream>>>(y, w1, b1, hid);
  k_w2<<<B, 512, 0, stream>>>(hid, w2, b2, hh, (u16*)d_out);
}
```

```cpp
#include <hip/hip_runtime.h>
#include <stdint.h>

typedef unsigned short u16;
typedef unsigned v4u __attribute__((ext_vector_type(4)));
typedef unsigned v2u __attribute__((ext_vector_type(2)));
typedef float v4f __attribute__((ext_vector_type(4)));
typedef short v4s __attribute__((ext_vector_type(4)));
typedef __bf16 v8b __attribute__((ext_vector_type(8)));
#define LAS __attribute__((address_space(3)))
#define DEV __device__ __forceinline__

constexpr int WS_EP = 0;
constexpr int WS_ST = 4096;
constexpr int WS_HH = WS_ST + 32768;
constexpr int WS_HD = WS_HH + 16384;
constexpr int WS_QP = WS_HD + 57344;
constexpr int WS_OP = WS_QP + 524288;
constexpr int WS_AP = WS_OP + 524288;
constexpr int WS_END = WS_AP + 524288;
constexpr int L_K = 0, L_V = 65536, L_W = 131072, L_M = 147456;
constexpr int L_XLN = L_M, L_OB = L_M, L_QL = L_M + 2048, L_RR = L_M + 2048, L_PF = L_M + 3072, L_TQ = L_PF, L_Y = L_PF, L_HB = L_PF;
constexpr int L_RMAX = L_M + 11264, L_RSUM = L_M + 11392, L_F = L_M + 11520, L_FLG = L_M + 11552, L_HS = L_M + 11648;
constexpr int LDS_BYTES = 163840;
constexpr int AUX_SC1 = 16;
constexpr unsigned SPIN_MAX = 200000u;

#define STAMP(i) do {} while (0)

DEV float bfu(unsigned hi16) { return __uint_as_float(hi16 << 16); }
DEV float lo_f(unsigned p) { return __uint_as_float(p << 16); }
DEV float hi_f(unsigned p) { return __uint_as_float(p & 0xffff0000u); }
typedef __bf16 v2b __attribute__((ext_vector_type(2)));
typedef float v2f __attribute__((ext_vector_type(2)));
DEV unsigned pk2(float lo, float hi) { return __builtin_bit_cast(unsigned, __builtin_convertvector(v2f{lo, hi}, v2b)); }
DEV float rb(float f) { return lo_f(pk2(f, f)); }
DEV unsigned enc(float v, unsigned t) { return ((__float_as_uint(v) + 0x80u) & 0xffffff00u) | t; }
DEV float dec(unsigned u) { return __uint_as_float(u & 0xffffff00u); }
DEV bool tag_ok(v4u x, unsigned t) { return ((((x.x ^ t) | (x.y ^ t)) | ((x.z ^ t) | (x.w ^ t))) & 0xffu) == 0; }
template <int C> DEV float dpp(float x) { return __builtin_bit_cast(float, __builtin_amdgcn_update_dpp(0, __builtin_bit_cast(int, x), C, 0xf, 0xf, true)); }
DEV float x16_sum(float x) { auto s = __builtin_amdgcn_permlane16_swap(__float_as_uint(x), __float_as_uint(x), false, false); return __uint_as_float(s[0]) + __uint_as_float(s[1]); }
DEV float x32_sum(float x) { auto s = __builtin_amdgcn_permlane32_swap(__float_as_uint(x), __float_as_uint(x), false, false); return __uint_as_float(s[0]) + __uint_as_float(s[1]); }
DEV float x16_max(float x) { auto s = __builtin_amdgcn_permlane16_swap(__float_as_uint(x), __float_as_uint(x), false, false); return fmaxf(__uint_as_float(s[0]), __uint_as_float(s[1])); }
DEV float x32_max(float x) { auto s = __builtin_amdgcn_permlane32_swap(__float_as_uint(x), __float_as_uint(x), false, false); return fmaxf(__uint_as_float(s[0]), __uint_as_float(s[1])); }
DEV float sum8(float x) { x += dpp<0xB1>(x); x += dpp<0x4E>(x); x += dpp<0x141>(x); return x; }
DEV float sum64(float x) { x = sum8(x); x += dpp<0x140>(x); x = x16_sum(x); return x32_sum(x); }
DEV void glds_nt(const void* g, unsigned lds) {
  unsigned keep;
  asm volatile("s_mov_b32 %0, m0\n\ts_mov_b32 m0, %2\n\ts_nop 0\n\tglobal_load_lds_dwordx4 %1, off nt\n\ts_mov_b32 m0, %0" : "=&s"(keep) : "v"(g), "s"(lds) : "memory");
}
DEV void glds(const void* g, unsigned lds) {
  unsigned keep;
  asm volatile("s_mov_b32 %0, m0\n\ts_mov_b32 m0, %2\n\ts_nop 0\n\tglobal_load_lds_dwordx4 %1, off\n\ts_mov_b32 m0, %0" : "=&s"(keep) : "v"(g), "s"(lds) : "memory");
}
#define BAR() asm volatile("s_waitcnt lgkmcnt(0)\n\ts_barrier" ::: "memory")
#define VMCNT(n) asm volatile("s_waitcnt vmcnt(" #n ")" ::: "memory")
#define FENCE() asm volatile("" ::: "memory")
DEV v8b mk(v4u a) { return __builtin_bit_cast(v8b, a); }
DEV v4f mfma(v4u a, v4u b, v4f c) { return __builtin_amdgcn_mfma_f32_16x16x32_bf16(mk(a), mk(b), c, 0, 0, 0); }
DEV v4u trfrag(LAS char* p, int off2) {
  v4s a = __builtin_amdgcn_ds_read_tr16_b64_v4i16((LAS v4s*)p);
  v4s b = __builtin_amdgcn_ds_read_tr16_b64_v4i16((LAS v4s*)(p + off2));
  v2u a2 = __builtin_bit_cast(v2u, a), b2 = __builtin_bit_cast(v2u, b);
  return v4u{a2.x, a2.y, b2.x, b2.y};
}
DEV void lds_wait(LAS unsigned* p, unsigned v) {
  for (unsigned s = 0; s < 50000000u; ++s) { if (*(volatile LAS unsigned*)p >= v) break; __builtin_amdgcn_s_sleep(1); }
  FENCE();
}
DEV void lds_inc(LAS unsigned* p, int lane) {
  asm volatile("s_waitcnt lgkmcnt(0)" ::: "memory");
  if (lane == 0) __hip_atomic_fetch_add(p, 1u, __ATOMIC_RELAXED, __HIP_MEMORY_SCOPE_WORKGROUP);
  FENCE();
}
DEV float bperm(int srclane, float v) { return __builtin_bit_cast(float, __builtin_amdgcn_ds_bpermute(srclane << 2, __builtin_bit_cast(int, v))); }
DEV float gelu_t(float z) {
  float u = 0.7978845608028654f * (z + 0.044715f * z * z * z);
  float e = __builtin_amdgcn_exp2f(-2.885390081777927f * u);
  return z * __builtin_amdgcn_rcpf(1.0f + e);
}

__global__ __launch_bounds__(640) void fused_decode_kernel(const u16* __restrict__ x, const u16* __restrict__ kc, const u16* __restrict__ vc,
    const u16* __restrict__ wq, const u16* __restrict__ wo, const u16* __restrict__ w1, const u16* __restrict__ b1, const u16* __restrict__ w2,
    const u16* __restrict__ b2, u16* __restrict__ out, char* ws
    ) {
  __shared__ __attribute__((aligned(1024))) char smem_[LDS_BYTES];
  LAS char* sm = (LAS char*)smem_;
  const unsigned lds0 = (unsigned)(size_t)sm;
  const int lane = threadIdx.x & 63, w = __builtin_amdgcn_readfirstlane(threadIdx.x >> 6);
  const int c = blockIdx.x, xc = c & 7, r = c >> 3;
  const int bq = r >> 2, sp = r & 3;
  const int cl = lane & 15, g = lane >> 4;
  __amdgpu_buffer_rsrc_t rs = __builtin_amdgcn_make_buffer_rsrc(ws, 0, WS_END, 0x00020000);
  LAS unsigned* flg = (LAS unsigned*)(sm + L_FLG);
  STAMP(0);
  const bool isR = r < 16, isM = (r >= 16) && (r < 30);
  const int bpR = r >> 2, cgR = 4 * xc + (r & 3);
  const int im = 14 * xc + (r - 16), hgM = im >> 2, bpM = im & 3;
  const int tq = (lane >> 2) & 3, tp = lane & 3;
  const int wf = ((tq >> 1) & 1) | ((g & 1) << 1);
  const int wbase = (8 * g + tq) * 128 + tp * 8;
  unsigned tag = 0;

  if (w == 8) {
    __builtin_amdgcn_s_setprio(3);
    unsigned E = __builtin_amdgcn_raw_buffer_load_b32(rs, WS_EP, 0, AUX_SC1);
    {
      const int kq = r & 3, cg = r >> 2;
      const char* src = (const char*)wq + (size_t)(128 * kq + (lane >> 3)) * 8192 + (512 * xc + 64 * cg) * 2;
#pragma unroll
      for (int j = 0; j < 16; ++j) {
        int f = ((lane >> 4) & 1) | ((j & 1) << 1);
        glds_nt(src + (size_t)j * 8 * 8192 + (((lane & 7) ^ (f << 1)) << 4), lds0 + L_W + j * 1024);
      }
    }
    if (lane < 8) flg[lane] = 0;
    BAR();
    VMCNT(0);
    STAMP(1);
    E = __builtin_amdgcn_readfirstlane(E);
    tag = (E + 1) & 0xff;
    if (lane == 0) flg[4] = tag | 0x100;
    lds_wait(flg + 0, 1);
    {
      v4u xb[4];
#pragma unroll
      for (int st = 0; st < 4; ++st) xb[st] = *(LAS v4u*)(sm + L_XLN + (cl & 7) * 256 + st * 64 + g * 16);
      v4f acc[4];
#pragma unroll
      for (int t = 0; t < 4; ++t) {
        acc[t] = v4f{0, 0, 0, 0};
        LAS char* p = sm + L_W + wbase + ((t ^ wf) << 5);
#pragma unroll
        for (int st = 0; st < 4; ++st) acc[t] = mfma(trfrag(p + st * 4096, 512), xb[st], acc[t]);
      }
      if (cl < 8) {
#pragma unroll
        for (int t = 0; t < 4; ++t) *(LAS v4f*)(sm + L_TQ + cl * 272 + (16 * t + 4 * g) * 4) = acc[t];
      }
      FENCE();
      const int kq = r & 3, cg = r >> 2;
#pragma unroll
      for (int i = 0; i < 2; ++i) {
        int idx = 64 * i + lane, b = idx >> 4, u = idx & 15;
        v4f v = *(LAS v4f*)(sm + L_TQ + b * 272 + u * 16);
        v4u o = {enc(v.x, tag), enc(v.y, tag), enc(v.z, tag), enc(v.w, tag)};
        __builtin_amdgcn_raw_buffer_store_b128(o, rs, WS_QP + ((xc * 8 + b) * 4 + kq) * 2048 + cg * 256 + u * 16, 0, AUX_SC1);
      }
    }
    STAMP(2);
    {
      v4u q[8];
      const int base = WS_QP + (xc * 8 + bq) * 8192 + lane * 16;
      for (unsigned sn = 0; sn < SPIN_MAX; ++sn) {
        FENCE();
        bool ok = true;
#pragma unroll
        for (int i = 0; i < 8; ++i) q[i] = __builtin_amdgcn_raw_buffer_load_b128(rs, base + i * 1024, 0, AUX_SC1);
#pragma unroll
        for (int i = 0; i < 8; ++i) ok &= tag_ok(q[i], tag);
        if (__all(ok)) break;
      }
#pragma unroll
      for (int hf = 0; hf < 2; ++hf) {
        float a0 = (dec(q[hf].x) + dec(q[2 + hf].x)) + (dec(q[4 + hf].x) + dec(q[6 + hf].x));
        float a1 = (dec(q[hf].y) + dec(q[2 + hf].y)) + (dec(q[4 + hf].y) + dec(q[6 + hf].y));
        float a2 = (dec(q[hf].z) + dec(q[2 + hf].z)) + (dec(q[4 + hf].z) + dec(q[6 + hf].z));
        float a3 = (dec(q[hf].w) + dec(q[2 + hf].w)) + (dec(q[4 + hf].w) + dec(q[6 + hf].w));
        *(LAS v2u*)(sm + L_QL + (hf * 256 + 4 * lane) * 2) = v2u{pk2(a0, a1), pk2(a2, a3)};
      }
      lds_inc(flg + 1, lane);
    }
    STAMP(3);
    lds_wait(flg + 3, 8);
    {
      const int h = lane & 3;
      v4f m0 = *(LAS v4f*)(sm + L_RMAX + h * 32), m1 = *(LAS v4f*)(sm + L_RMAX + h * 32 + 16);
      v4f s0 = *(LAS v4f*)(sm + L_RSUM + h * 32), s1 = *(LAS v4f*)(sm + L_RSUM + h * 32 + 16);
      float m = fmaxf(fmaxf(fmaxf(m0.x, m0.y), fmaxf(m0.z, m0.w)), fmaxf(fmaxf(m1.x, m1.y), fmaxf(m1.z, m1.w)));
      float l = ((s0.x + s0.y) + (s0.z + s0.w)) + ((s1.x + s1.y) + (s1.z + s1.w));
      float v = (lane & 4) ? l : m;
      v4u o = {enc(dpp<0x00>(v), tag), enc(dpp<0x55>(v), tag), enc(dpp<0xAA>(v), tag), enc(dpp<0xFF>(v), tag)};
      const int sb = WS_ST + (xc * 8 + bq) * 512;
      if (lane == 0 || lane == 4) __builtin_amdgcn_raw_buffer_store_b128(o, rs, sb + sp * 128 + (lane >> 2) * 16, 0, AUX_SC1);
      STAMP(4);
      v4u mu, lu;
      for (unsigned sn = 0; sn < SPIN_MAX; ++sn) {
        FENCE();
        mu = __builtin_amdgcn_raw_buffer_load_b128(rs, sb + (lane & 3) * 128, 0, AUX_SC1);
        lu = __builtin_amdgcn_raw_buffer_load_b128(rs, sb + (lane & 3) * 128 + 16, 0, AUX_SC1);
        if (__all(tag_ok(mu, tag) && tag_ok(lu, tag))) break;
      }
      float mv[4] = {dec(mu.x), dec(mu.y), dec(mu.z), dec(mu.w)}, lv[4] = {dec(lu.x), dec(lu.y), dec(lu.z), dec(lu.w)}, fv[4];
#pragma unroll
      for (int i = 0; i < 4; ++i) {
        float M = fmaxf(mv[i], dpp<0xB1>(mv[i])); M = fmaxf(M, dpp<0x4E>(M));
        float sc = __builtin_amdgcn_exp2f((mv[i] - M) * 1.4426950408889634f);
        float L = lv[i] * sc; L += dpp<0xB1>(L); L += dpp<0x4E>(L);
        fv[i] = sc * __builtin_amdgcn_rcpf(L);
      }
      if (lane == sp) *(LAS v4f*)(sm + L_F) = v4f{fv[0], fv[1], fv[2], fv[3]};
    }
    STAMP(5);
    v2u xres = {0, 0};
    if (isR) xres = *(const v2u*)(x + (2 * bpR + ((lane >> 2) & 1)) * 512 + 16 * cgR + 4 * (lane & 3));
    BAR();
    BAR();
    BAR();
    if (isR) {
      v4u a[4];
      const int base = WS_AP + (bpR * 32 + cgR) * 4096 + lane * 16;
      for (unsigned sn = 0; sn < SPIN_MAX; ++sn) {
        FENCE();
        bool ok = true;
#pragma unroll
        for (int i = 0; i < 4; ++i) a[i] = __builtin_amdgcn_raw_buffer_load_b128(rs, base + i * 1024, 0, AUX_SC1);
#pragma unroll
        for (int i = 0; i < 4; ++i) ok &= tag_ok(a[i], tag);
        if (__all(ok)) break;
      }
      STAMP(9);
      float s[4] = {(dec(a[0].x) + dec(a[1].x)) + (dec(a[2].x) + dec(a[3].x)), (dec(a[0].y) + dec(a[1].y)) + (dec(a[2].y) + dec(a[3].y)),
                    (dec(a[0].z) + dec(a[1].z)) + (dec(a[2].z) + dec(a[3].z)), (dec(a[0].w) + dec(a[1].w)) + (dec(a[2].w) + dec(a[3].w))};
#pragma unroll
      for (int i = 0; i < 4; ++i) { s[i] += dpp<0x128>(s[i]); s[i] = x16_sum(s[i]); s[i] = x32_sum(s[i]); }
      float h0 = rb(lo_f(xres.x) + rb(s[0])), h1 = rb(hi_f(xres.x) + rb(s[1])), h2 = rb(lo_f(xres.y) + rb(s[2])), h3 = rb(hi_f(xres.y) + rb(s[3]));
      v4u o = {__float_as_uint(h0) | tag, __float_as_uint(h1) | tag, __float_as_uint(h2) | tag, __float_as_uint(h3) | tag};
      if (lane < 8) {
        __builtin_amdgcn_raw_buffer_store_b128(o, rs, WS_HH + (bpR * 32 + cgR) * 128 + lane * 16, 0, AUX_SC1);
        *(LAS v4f*)(sm + L_HS + lane * 16) = v4f{h0, h1, h2, h3};
      }
      STAMP(10);
      BAR();
      BAR();
      if (c == 0 && lane == 0) __builtin_amdgcn_raw_buffer_store_b32(E + 1, rs, WS_EP, 0, AUX_SC1);
    } else if (isM) {
      BAR();
      BAR();
    }
    return;
  }
  if (w == 9) {
    v4u xr[8], xs[2];
    const u16* xp = x + (lane >> 3) * 512 + (lane & 7) * 8;
#pragma unroll
    for (int i = 0; i < 8; ++i) xr[i] = *(const v4u*)(xp + 64 * i);
#pragma unroll
    for (int i = 0; i < 2; ++i) xs[i] = *(const v4u*)(xp + 128 * (r & 3) + 64 * i);
    BAR();
    {
      float s = 0;
#pragma unroll
      for (int i = 0; i < 8; ++i)
#pragma unroll
        for (int j = 0; j < 4; ++j) s += lo_f(xr[i][j]) + hi_f(xr[i][j]);
      float mean = sum8(s) * (1.0f / 512.0f);
      float v = 0;
#pragma unroll
      for (int i = 0; i < 8; ++i)
#pragma unroll
        for (int j = 0; j < 4; ++j) { float d0 = lo_f(xr[i][j]) - mean, d1 = hi_f(xr[i][j]) - mean; v += d0 * d0 + d1 * d1; }
      float var = sum8(v) * (1.0f / 512.0f);
      float mb = rb(mean), den = rb(sqrtf(rb(rb(var) + 1e-5f)));
      float inv = 1.0f / den;
#pragma unroll
      for (int i = 0; i < 2; ++i) {
        v4u o;
#pragma unroll
        for (int j = 0; j < 4; ++j) o[j] = pk2(rb(lo_f(xs[i][j]) - mb) * inv, rb(hi_f(xs[i][j]) - mb) * inv);
        *(LAS v4u*)(sm + L_XLN + (lane >> 3) * 256 + (64 * i + (lane & 7) * 8) * 2) = o;
      }
      lds_inc(flg + 0, lane);
    }
    {
      const char* vb = (const char*)vc + ((size_t)(bq * 8 + xc) * 4096 + 3072 + sp * 256) * 256;
      const int pos = lane & 15;
      const int o0 = g * 256 + ((((pos >> 1) ^ g) << 5) | ((pos & 1) << 4));
      const int o1 = g * 256 + ((((pos >> 1) ^ (4 + g)) << 5) | ((pos & 1) << 4));
#pragma unroll 8
      for (int j = 0; j < 64; ++j) glds_nt(vb + j * 1024 + ((j & 1) ? o1 : o0), lds0 + L_V + j * 1024);
    }
    STAMP(1);
    lds_wait(flg + 1, 1);
    {
      const int qp = r >> 3, cg = r & 7, e = lane >> 3;
      const char* src = (const char*)wo + (size_t)(512 * xc + 128 * (e & 3) + 32 * qp + (e >> 2)) * 1024 + cg * 128;
#pragma unroll
      for (int j = 0; j < 16; ++j) {
        int f = ((lane >> 4) & 1) | ((j & 1) << 1);
        glds_nt(src + (size_t)(8 * (j >> 2) + 2 * (j & 3)) * 1024 + (((lane & 7) ^ (f << 1)) << 4), lds0 + L_W + j * 1024);
      }
    }
    VMCNT(16);
    STAMP(2);
    BAR();
    VMCNT(0);
    STAMP(3);
    BAR();
    return;
  }
  v2u b1v = {0, 0};
  unsigned b2v = 0;
  if (isM && w < 4) b1v = *(const v2u*)(b1 + 64 * hgM + 16 * w + 4 * g);
  if (isR && w == 0) b2v = b2[16 * cgR + cl];
  BAR();
  {
    const char* kb = (const char*)kc + ((size_t)(bq * 8 + xc) * 4096 + 3072 + sp * 256 + 32 * w) * 256;
#pragma unroll
    for (int i = 0; i < 8; ++i) {
      int key = 4 * i + g;
      glds_nt(kb + key * 256 + (((lane & 15) ^ (key & 15)) << 4), lds0 + L_K + (32 * w + 4 * i) * 256);
    }
  }
  VMCNT(0);
  STAMP(1);
  lds_wait(flg + 1, 1);
  STAMP(2);
  {
    v4f acc[2] = {v4f{0, 0, 0, 0}, v4f{0, 0, 0, 0}};
#pragma unroll
    for (int st = 0; st < 4; ++st) {
      v4u qf = *(LAS v4u*)(sm + L_QL + (cl & 3) * 256 + st * 64 + g * 16);
#pragma unroll
      for (int tt = 0; tt < 2; ++tt) {
        v4u kf = *(LAS v4u*)(sm + L_K + (32 * w + 16 * tt + cl) * 256 + (((4 * st + g) ^ cl) << 4));
        acc[tt] = mfma(kf, qf, acc[tt]);
      }
    }
    float s[8];
#pragma unroll
    for (int i = 0; i < 8; ++i) s[i] = rb(rb(acc[i >> 2][i & 3]) * 0.08838834764831845f);
    float mx = fmaxf(fmaxf(fmaxf(s[0], s[1]), fmaxf(s[2], s[3])), fmaxf(fmaxf(s[4], s[5]), fmaxf(s[6], s[7])));
    mx = x32_max(x16_max(mx));
    if (lane < 4) *(LAS float*)(sm + L_RMAX + lane * 32 + w * 4) = mx;
    lds_inc(flg + 2, lane);
    lds_wait(flg + 2, 8);
    v4f m0 = *(LAS v4f*)(sm + L_RMAX + (cl & 3) * 32), m1 = *(LAS v4f*)(sm + L_RMAX + (cl & 3) * 32 + 16);
    float m = fmaxf(fmaxf(fmaxf(m0.x, m0.y), fmaxf(m0.z, m0.w)), fmaxf(fmaxf(m1.x, m1.y), fmaxf(m1.z, m1.w)));
    float ls = 0;
#pragma unroll
    for (int i = 0; i < 8; ++i) { s[i] = __builtin_amdgcn_exp2f((s[i] - m) * 1.4426950408889634f); ls += s[i]; }
    ls = x32_sum(x16_sum(ls));
    if (lane < 4) *(LAS float*)(sm + L_RSUM + lane * 32 + w * 4) = ls;
    *(LAS v4u*)(sm + L_PF + w * 1024 + lane * 16) = v4u{pk2(s[0], s[1]), pk2(s[2], s[3]), pk2(s[4], s[5]), pk2(s[6], s[7])};
    lds_inc(flg + 3, lane);
  }
  STAMP(3);
  lds_wait(flg + 3, 8);
  v4u pf[8];
#pragma unroll
  for (int m = 0; m < 8; ++m) pf[m] = *(LAS v4u*)(sm + L_PF + m * 1024 + lane * 16);
  BAR();
  STAMP(5);
  tag = flg[4] & 0xff;
  {
    v4f fv = *(LAS v4f*)(sm + L_F);
    LAS char* p = sm + L_V + (4 * g + tq) * 256 + ((w ^ (4 * (g & 1) + tq)) << 5) + tp * 8;
    v4f acc = {0, 0, 0, 0};
#pragma unroll
    for (int m = 0; m < 8; ++m) acc = mfma(pf[m], trfrag(p + m * 8192, 4096), acc);
    v4u o = {enc(acc.x * fv.x, tag), enc(acc.y * fv.y, tag), enc(acc.z * fv.z, tag), enc(acc.w * fv.w, tag)};
    if (lane < 16)
      __builtin_amdgcn_raw_buffer_store_b128(o, rs, WS_OP + (((xc * 4 + (w >> 1)) * 8 + bq) * 4 + sp) * 512 + ((w & 1) * 16 + lane) * 16, 0, AUX_SC1);
  }
  STAMP(6);
  BAR();
  v4u wof[4];
  if (w < 4) {
    LAS char* p = sm + L_W + wbase + ((w ^ wf) << 5);
#pragma unroll
    for (int m = 0; m < 4; ++m) wof[m] = trfrag(p + m * 4096, 512);
  }
  {
    v4u a[2];
    const int base = WS_OP + ((xc * 4 + (r >> 3)) * 8 + w) * 2048 + lane * 16;
    for (unsigned sn = 0; sn < SPIN_MAX; ++sn) {
      FENCE();
      a[0] = __builtin_amdgcn_raw_buffer_load_b128(rs, base, 0, AUX_SC1);
      a[1] = __builtin_amdgcn_raw_buffer_load_b128(rs, base + 1024, 0, AUX_SC1);
      if (__all(tag_ok(a[0], tag) && tag_ok(a[1], tag))) break;
    }
    STAMP(7);
    float o0 = x32_sum(dec(a[0].x) + dec(a[1].x)), o1 = x32_sum(dec(a[0].y) + dec(a[1].y));
    float o2 = x32_sum(dec(a[0].z) + dec(a[1].z)), o3 = x32_sum(dec(a[0].w) + dec(a[1].w));
    if (lane < 32) *(LAS v2u*)(sm + L_OB + (lane >> 3) * 512 + ((lane & 7) >> 1) * 128 + w * 16 + (lane & 1) * 8) = v2u{pk2(o0, o1), pk2(o2, o3)};
  }
  BAR();
  if (w < 4) {
    v4f acc = {0, 0, 0, 0};
#pragma unroll
    for (int m = 0; m < 4; ++m) acc = mfma(wof[m], *(LAS v4u*)(sm + L_OB + m * 512 + g * 128 + (cl & 7) * 16), acc);
    const int src = 2 * (lane >> 3) + ((lane >> 2) & 1) + 16 * (lane & 3);
    v4u o = {enc(bperm(src, acc.x), tag), enc(bperm(src, acc.y), tag), enc(bperm(src, acc.z), tag), enc(bperm(src, acc.w), tag)};
    if (lane < 32)
      __builtin_amdgcn_raw_buffer_store_b128(o, rs, WS_AP + (((lane >> 3) * 32 + 4 * (r & 7) + w) * 32 + (xc * 4 + (r >> 3))) * 128 + (lane & 7) * 16, 0, AUX_SC1);
  }
  STAMP(8);
  if (isM) {
    {
      const char* src = (const char*)w1 + (size_t)(64 * w + (lane >> 3)) * 3584 + hgM * 128;
#pragma unroll
      for (int j = 0; j < 8; ++j) {
        int f = ((lane >> 4) & 1) | ((j & 1) << 1);
        glds(src + (size_t)j * 8 * 3584 + (((lane & 7) ^ (f << 1)) << 4), lds0 + L_K + (8 * w + j) * 1024);
      }
    }
    VMCNT(0);
    STAMP(9);
    BAR();
    if (w < 4) {
      v4u af[16];
      LAS char* p = sm + L_K + wbase + ((w ^ wf) << 5);
#pragma unroll
      for (int st = 0; st < 16; ++st) af[st] = trfrag(p + st * 4096, 512);
      BAR();
      STAMP(11);
      v4f acc = {0, 0, 0, 0};
#pragma unroll
      for (int st = 0; st < 16; ++st) acc = mfma(af[st], *(LAS v4u*)(sm + L_Y + (cl & 1) * 1024 + st * 64 + g * 16), acc);
      float h0 = rb(gelu_t(rb(rb(acc.x) + lo_f(b1v.x)))), h1 = rb(gelu_t(rb(rb(acc.y) + hi_f(b1v.x))));
      float h2 = rb(gelu_t(rb(rb(acc.z) + lo_f(b1v.y)))), h3 = rb(gelu_t(rb(rb(acc.w) + hi_f(b1v.y))));
      const int src = ((lane >> 2) & 1) + 16 * (lane & 3);
      v4u o = {__float_as_uint(bperm(src, h0)) | tag, __float_as_uint(bperm(src, h1)) | tag, __float_as_uint(bperm(src, h2)) | tag, __float_as_uint(bperm(src, h3)) | tag};
      if (lane < 8) __builtin_amdgcn_raw_buffer_store_b128(o, rs, WS_HD + (bpM * 112 + 4 * hgM + w) * 128 + lane * 16, 0, AUX_SC1);
      STAMP(12);
    } else if (w < 6) {
      const int bl = w - 4;
      v4u a[2];
      const int base = WS_HH + bpM * 4096 + (lane >> 2) * 128 + bl * 64 + (lane & 3) * 16;
      for (unsigned sn = 0; sn < SPIN_MAX; ++sn) {
        FENCE();
        a[0] = __builtin_amdgcn_raw_buffer_load_b128(rs, base, 0, AUX_SC1);
        a[1] = __builtin_amdgcn_raw_buffer_load_b128(rs, base + 2048, 0, AUX_SC1);
        if (__all(tag_ok(a[0], tag) && tag_ok(a[1], tag))) break;
      }
      STAMP(10);
      float v[8] = {dec(a[0].x), dec(a[0].y), dec(a[0].z), dec(a[0].w), dec(a[1].x), dec(a[1].y), dec(a[1].z), dec(a[1].w)};
#pragma unroll
      for (int pass = 0; pass < 2; ++pass) {
        float s = ((v[0] + v[1]) + (v[2] + v[3])) + ((v[4] + v[5]) + (v[6] + v[7]));
        float mean = sum64(s) * (1.0f / 512.0f);
        float q = 0;
#pragma unroll
        for (int i = 0; i < 8; ++i) { float d = v[i] - mean; q += d * d; }
        float var = sum64(q) * (1.0f / 512.0f);
        float mb = rb(mean), inv = 1.0f / rb(sqrtf(rb(rb(var) + 1e-5f)));
#pragma unroll
        for (int i = 0; i < 8; ++i) v[i] = rb(rb(v[i] - mb) * inv);
      }
      *(LAS v2u*)(sm + L_Y + bl * 1024 + lane * 8) = v2u{pk2(v[0], v[1]), pk2(v[2], v[3])};
      *(LAS v2u*)(sm + L_Y + bl * 1024 + 512 + lane * 8) = v2u{pk2(v[4], v[5]), pk2(v[6], v[7])};
      BAR();
    } else {
      BAR();
    }
  } else if (isR) {
    BAR();
    {
      const int rho = lane >> 1, gg = rho >> 3, e = rho & 7;
      const int hid = (e < 4) ? (4 * gg + e) : (16 + 4 * gg + e - 4);
      const char* src = (const char*)w2 + (size_t)(32 * 7 * w + hid) * 1024 + cgR * 32 + (lane & 1) * 16;
#pragma unroll
      for (int j = 0; j < 7; ++j) glds(src + (size_t)j * 32 * 1024, lds0 + L_K + (7 * w + j) * 1024);
    }
    VMCNT(0);
    STAMP(9);
    v4u af[7];
    {
      LAS char* p = sm + L_K + 7 * w * 1024 + (8 * g + tq) * 32 + tp * 8;
#pragma unroll
      for (int m = 0; m < 7; ++m) af[m] = trfrag(p + m * 1024, 128);
    }
    v4u a[2];
    const int base = WS_HD + (bpR * 112 + 14 * w) * 128;
    const int o1 = (lane < 48 ? 64 + lane : lane) * 16;
    for (unsigned sn = 0; sn < SPIN_MAX; ++sn) {
      FENCE();
      a[0] = __builtin_amdgcn_raw_buffer_load_b128(rs, base + lane * 16, 0, AUX_SC1);
      a[1] = __builtin_amdgcn_raw_buffer_load_b128(rs, base + o1, 0, AUX_SC1);
      if (__all(tag_ok(a[0], tag) && tag_ok(a[1], tag))) break;
    }
    STAMP(11);
    {
      LAS char* hb = sm + L_HB + w * 896;
      const int u0 = lane, u1 = 64 + lane;
      *(LAS v2u*)(hb + (u0 >> 4) * 128 + (u0 & 3) * 32 + ((u0 >> 2) & 1) * 16 + ((u0 >> 3) & 1) * 8) =
          v2u{(a[0].x >> 16) | (a[0].y & 0xffff0000u), (a[0].z >> 16) | (a[0].w & 0xffff0000u)};
      if (lane < 48)
        *(LAS v2u*)(hb + (u1 >> 4) * 128 + (u1 & 3) * 32 + ((u1 >> 2) & 1) * 16 + ((u1 >> 3) & 1) * 8) =
            v2u{(a[1].x >> 16) | (a[1].y & 0xffff0000u), (a[1].z >> 16) | (a[1].w & 0xffff0000u)};
      FENCE();
      v4f acc = {0, 0, 0, 0};
#pragma unroll
      for (int m = 0; m < 7; ++m) acc = mfma(af[m], *(LAS v4u*)(hb + m * 128 + g * 32 + (cl & 1) * 16), acc);
      if (cl < 2) *(LAS v4f*)(sm + L_RR + w * 128 + cl * 64 + g * 16) = acc;
    }
    BAR();
    if (w == 0 && lane < 32) {
      float s = 0;
#pragma unroll
      for (int i = 0; i < 8; ++i) s += *(LAS float*)(sm + L_RR + i * 128 + lane * 4);
      float h = *(LAS float*)(sm + L_HS + lane * 4);
      float o = h + rb(rb(s) + bfu(b2v));
      out[(2 * bpR + (lane >> 4)) * 512 + 16 * cgR + cl] = (u16)(pk2(o, o) & 0xffff);
    }
    STAMP(12);
  }
}

extern "C" void kernel_launch(void* const* d_in, const int* in_sizes, int n_in, void* d_out, int out_size, void* d_ws, size_t ws_size, hipStream_t stream) {
  fused_decode_kernel<<<256, 640, 0, stream>>>((const u16*)d_in[0], (const u16*)d_in[1], (const u16*)d_in[2], (const u16*)d_in[3], (const u16*)d_in[4],
                                               (const u16*)d_in[6], (const u16*)d_in[7], (const u16*)d_in[8], (const u16*)d_in[9], (u16*)d_out, (char*)d_ws);
}
```

```cpp
#include <hip/hip_runtime.h>
#include <stdint.h>

typedef unsigned short u16;
typedef unsigned v4u __attribute__((ext_vector_type(4)));
typedef unsigned v2u __attribute__((ext_vector_type(2)));
typedef float v4f __attribute__((ext_vector_type(4)));
typedef short v4s __attribute__((ext_vector_type(4)));
typedef __bf16 v8b __attribute__((ext_vector_type(8)));
#define LAS __attribute__((address_space(3)))
#define DEV __device__ __forceinline__

constexpr int WS_EP = 0;
constexpr int WS_ST = 4096;
constexpr int WS_HH = WS_ST + 32768;
constexpr int WS_QP = WS_HH + 16384;
constexpr int WS_OP = WS_QP + 524288;
constexpr int WS_AP = WS_OP + 524288;
constexpr int WS_FP = WS_AP + 524288;
constexpr int WS_END = WS_FP + 128 * 28 * 128;
constexpr int L_K = 0, L_V = 65536, L_W = 131072, L_M = 147456;
constexpr int L_XLN = L_M, L_OB = L_M, L_QL = L_M + 2048, L_PF = L_M + 3072, L_TQ = L_PF, L_Y = L_PF, L_TB = L_PF + 2048, L_HB = L_M + 2048;
constexpr int L_RMAX = L_M + 11264, L_RSUM = L_M + 11392, L_FLG = L_M + 11552;
constexpr int LDS_BYTES = 163840;
constexpr int AUX_SC1 = 16;
constexpr unsigned SPIN_MAX = 200000u;

#define STAMP(i) do {} while (0)
#define FLUSH() do {} while (0)

DEV float bfu(unsigned hi16) { return __uint_as_float(hi16 << 16); }
DEV float lo_f(unsigned p) { return __uint_as_float(p << 16); }
DEV float hi_f(unsigned p) { return __uint_as_float(p & 0xffff0000u); }
typedef __bf16 v2b __attribute__((ext_vector_type(2)));
typedef float v2f __attribute__((ext_vector_type(2)));
DEV unsigned pk2(float lo, float hi) { return __builtin_bit_cast(unsigned, __builtin_convertvector(v2f{lo, hi}, v2b)); }
DEV float rb(float f) { return lo_f(pk2(f, f)); }
DEV unsigned enc(float v, unsigned t) { return ((__float_as_uint(v) + 0x80u) & 0xffffff00u) | t; }
DEV float dec(unsigned u) { return __uint_as_float(u & 0xffffff00u); }
DEV bool tag_ok(v4u x, unsigned t) { return ((((x.x ^ t) | (x.y ^ t)) | ((x.z ^ t) | (x.w ^ t))) & 0xffu) == 0; }
template <int C> DEV float dpp(float x) { return __builtin_bit_cast(float, __builtin_amdgcn_update_dpp(0, __builtin_bit_cast(int, x), C, 0xf, 0xf, true)); }
DEV float x16_sum(float x) { auto s = __builtin_amdgcn_permlane16_swap(__float_as_uint(x), __float_as_uint(x), false, false); return __uint_as_float(s[0]) + __uint_as_float(s[1]); }
DEV float x32_sum(float x) { auto s = __builtin_amdgcn_permlane32_swap(__float_as_uint(x), __float_as_uint(x), false, false); return __uint_as_float(s[0]) + __uint_as_float(s[1]); }
DEV float x16_max(float x) { auto s = __builtin_amdgcn_permlane16_swap(__float_as_uint(x), __float_as_uint(x), false, false); return fmaxf(__uint_as_float(s[0]), __uint_as_float(s[1])); }
DEV float x32_max(float x) { auto s = __builtin_amdgcn_permlane32_swap(__float_as_uint(x), __float_as_uint(x), false, false); return fmaxf(__uint_as_float(s[0]), __uint_as_float(s[1])); }
DEV float sum8(float x) { x += dpp<0xB1>(x); x += dpp<0x4E>(x); x += dpp<0x141>(x); return x; }
DEV float sum64(float x) { x = sum8(x); x += dpp<0x140>(x); x = x16_sum(x); return x32_sum(x); }
DEV void glds_nt(const void* g, unsigned lds) {
  unsigned keep;
  asm volatile("s_mov_b32 %0, m0\n\ts_mov_b32 m0, %2\n\ts_nop 0\n\tglobal_load_lds_dwordx4 %1, off nt\n\ts_mov_b32 m0, %0" : "=&s"(keep) : "v"(g), "s"(lds) : "memory");
}
DEV void glds(const void* g, unsigned lds) {
  unsigned keep;
  asm volatile("s_mov_b32 %0, m0\n\ts_mov_b32 m0, %2\n\ts_nop 0\n\tglobal_load_lds_dwordx4 %1, off\n\ts_mov_b32 m0, %0" : "=&s"(keep) : "v"(g), "s"(lds) : "memory");
}
#define BAR() asm volatile("s_waitcnt lgkmcnt(0)\n\ts_barrier" ::: "memory")
#define VMCNT(n) asm volatile("s_waitcnt vmcnt(" #n ")" ::: "memory")
#define FENCE() asm volatile("" ::: "memory")
DEV v8b mk(v4u a) { return __builtin_bit_cast(v8b, a); }
DEV v4f mfma(v4u a, v4u b, v4f c) { return __builtin_amdgcn_mfma_f32_16x16x32_bf16(mk(a), mk(b), c, 0, 0, 0); }
DEV v4u trfrag(LAS char* p, int off2) {
  v4s a = __builtin_amdgcn_ds_read_tr16_b64_v4i16((LAS v4s*)p);
  v4s b = __builtin_amdgcn_ds_read_tr16_b64_v4i16((LAS v4s*)(p + off2));
  v2u a2 = __builtin_bit_cast(v2u, a), b2 = __builtin_bit_cast(v2u, b);
  return v4u{a2.x, a2.y, b2.x, b2.y};
}
DEV void lds_wait(LAS unsigned* p, unsigned v) {
  for (unsigned s = 0; s < 50000000u; ++s) { if (*(volatile LAS unsigned*)p >= v) break; }
  FENCE();
}
DEV void lds_inc(LAS unsigned* p, int lane) {
  asm volatile("s_waitcnt lgkmcnt(0)" ::: "memory");
  if (lane == 0) __hip_atomic_fetch_add(p, 1u, __ATOMIC_RELAXED, __HIP_MEMORY_SCOPE_WORKGROUP);
  FENCE();
}
DEV float bperm(int srclane, float v) { return __builtin_bit_cast(float, __builtin_amdgcn_ds_bpermute(srclane << 2, __builtin_bit_cast(int, v))); }
DEV float gelu_t(float z) {
  float u = 0.7978845608028654f * (z + 0.044715f * z * z * z);
  float e = __builtin_amdgcn_exp2f(-2.885390081777927f * u);
  return z * __builtin_amdgcn_rcpf(1.0f + e);
}

__global__ __launch_bounds__(640) void fused_decode_kernel(const u16* __restrict__ x, const u16* __restrict__ kc, const u16* __restrict__ vc,
    const u16* __restrict__ wq, const u16* __restrict__ wo, const u16* __restrict__ w1, const u16* __restrict__ b1, const u16* __restrict__ w2,
    const u16* __restrict__ b2, u16* __restrict__ out, char* ws
    ) {
  __shared__ __attribute__((aligned(1024))) char smem_[LDS_BYTES];
  LAS char* sm = (LAS char*)smem_;
  const unsigned lds0 = (unsigned)(size_t)sm;
  const int lane = threadIdx.x & 63, w = __builtin_amdgcn_readfirstlane(threadIdx.x >> 6);
  const int c = blockIdx.x, xc = c & 7, r = c >> 3;
  const int bq = r >> 2, sp = r & 3;
  const int cl = lane & 15, g = lane >> 4;
  __amdgpu_buffer_rsrc_t rs = __builtin_amdgcn_make_buffer_rsrc(ws, 0, WS_END, 0x00020000);
  LAS unsigned* flg = (LAS unsigned*)(sm + L_FLG);
  STAMP(0);
  const bool isR = r < 16, isM = (r >= 16) && (r < 30);
  const int bpR = r >> 2, cgR = 4 * xc + (r & 3);
  const int im = 14 * xc + (r - 16), hgM = im >> 2, bpM = im & 3;
  const int tq = (lane >> 2) & 3, tp = lane & 3;
  unsigned tag = 0;

  if (w == 8) {
    __builtin_amdgcn_s_setprio(3);
    unsigned E = __builtin_amdgcn_raw_buffer_load_b32(rs, WS_EP, 0, AUX_SC1);
    {
      const int kq = r & 3, cg = r >> 2;
      const char* src = (const char*)wq + (size_t)(128 * kq + (lane >> 3)) * 8192 + (512 * xc + 64 * cg) * 2;
      const int s0 = ((lane & 7) ^ (((lane >> 4) & 1) << 1)) << 4;
#pragma unroll
      for (int j = 0; j < 16; ++j) glds_nt(src + (size_t)j * 8 * 8192 + (s0 ^ ((j & 1) << 6)), lds0 + L_W + j * 1024);
    }
    if (lane < 8) flg[lane] = 0;
    BAR();
    VMCNT(0);
    STAMP(1);
    E = __builtin_amdgcn_readfirstlane(E);
    tag = (E + 1) & 0xff;
    if (lane == 0) flg[4] = tag | 0x100;
    lds_wait(flg + 0, 1);
    {
      v4u xb[4];
#pragma unroll
      for (int st = 0; st < 4; ++st) xb[st] = *(LAS v4u*)(sm + L_XLN + (cl & 7) * 256 + st * 64 + g * 16);
      v4f acc[4];
      const int wf = ((tq >> 1) & 1) | ((g & 1) << 1);
#pragma unroll
      for (int t = 0; t < 4; ++t) {
        acc[t] = v4f{0, 0, 0, 0};
        LAS char* p = sm + L_W + (8 * g + tq) * 128 + tp * 8 + ((t ^ wf) << 5);
#pragma unroll
        for (int st = 0; st < 4; ++st) acc[t] = mfma(trfrag(p + st * 4096, 512), xb[st], acc[t]);
      }
      if (cl < 8) {
#pragma unroll
        for (int t = 0; t < 4; ++t) *(LAS v4f*)(sm + L_TQ + cl * 272 + (16 * t + 4 * g) * 4) = acc[t];
      }
      FENCE();
      const int kq = r & 3, cg = r >> 2;
#pragma unroll
      for (int i = 0; i < 2; ++i) {
        int idx = 64 * i + lane, b = idx >> 4, u = idx & 15;
        v4f v = *(LAS v4f*)(sm + L_TQ + b * 272 + u * 16);
        v4u o = {enc(v.x, tag), enc(v.y, tag), enc(v.z, tag), enc(v.w, tag)};
        __builtin_amdgcn_raw_buffer_store_b128(o, rs, WS_QP + ((xc * 8 + b) * 4 + kq) * 2048 + cg * 256 + u * 16, 0, AUX_SC1);
      }
    }
    STAMP(2);
    {
      v4u q[8];
      const int base = WS_QP + (xc * 8 + bq) * 8192 + lane * 16;
      for (unsigned sn = 0; sn < SPIN_MAX; ++sn) {
        FENCE();
        bool ok = true;
#pragma unroll
        for (int i = 0; i < 8; ++i) q[i] = __builtin_amdgcn_raw_buffer_load_b128(rs, base + i * 1024, 0, AUX_SC1);
#pragma unroll
        for (int i = 0; i < 8; ++i) ok &= tag_ok(q[i], tag);
        if (__all(ok)) break;
      }
#pragma unroll
      for (int hf = 0; hf < 2; ++hf) {
        float a0 = (dec(q[hf].x) + dec(q[2 + hf].x)) + (dec(q[4 + hf].x) + dec(q[6 + hf].x));
        float a1 = (dec(q[hf].y) + dec(q[2 + hf].y)) + (dec(q[4 + hf].y) + dec(q[6 + hf].y));
        float a2 = (dec(q[hf].z) + dec(q[2 + hf].z)) + (dec(q[4 + hf].z) + dec(q[6 + hf].z));
        float a3 = (dec(q[hf].w) + dec(q[2 + hf].w)) + (dec(q[4 + hf].w) + dec(q[6 + hf].w));
        *(LAS v2u*)(sm + L_QL + (hf * 256 + 4 * lane) * 2) = v2u{pk2(a0, a1), pk2(a2, a3)};
      }
      lds_inc(flg + 1, lane);
    }
    STAMP(3);
    v2u xres = {0, 0};
    unsigned b2lo = 0, b2hi = 0;
    if (isR) {
      xres = *(const v2u*)(x + (2 * bpR + ((lane >> 2) & 1)) * 512 + 16 * cgR + 4 * (lane & 3));
      v2u bb = *(const v2u*)(b2 + 16 * cgR + 4 * (lane & 3));
      b2lo = bb.x; b2hi = bb.y;
    }
    lds_wait(flg + 3, 8);
    {
      const int h = lane & 3;
      v4f m0 = *(LAS v4f*)(sm + L_RMAX + h * 32), m1 = *(LAS v4f*)(sm + L_RMAX + h * 32 + 16);
      v4f s0 = *(LAS v4f*)(sm + L_RSUM + h * 32), s1 = *(LAS v4f*)(sm + L_RSUM + h * 32 + 16);
      float m = fmaxf(fmaxf(fmaxf(m0.x, m0.y), fmaxf(m0.z, m0.w)), fmaxf(fmaxf(m1.x, m1.y), fmaxf(m1.z, m1.w)));
      float l = ((s0.x + s0.y) + (s0.z + s0.w)) + ((s1.x + s1.y) + (s1.z + s1.w));
      float v = (lane & 4) ? l : m;
      v4u o = {enc(dpp<0x00>(v), tag), enc(dpp<0x55>(v), tag), enc(dpp<0xAA>(v), tag), enc(dpp<0xFF>(v), tag)};
      if (lane == 0 || lane == 4) __builtin_amdgcn_raw_buffer_store_b128(o, rs, WS_ST + (xc * 8 + bq) * 512 + sp * 128 + (lane >> 2) * 16, 0, AUX_SC1);
    }
    STAMP(4);
    BAR();
    if (isM) { BAR(); BAR(); BAR(); BAR(); FLUSH(); return; }
    if (!isR) { FLUSH(); return; }
    BAR();
    BAR();
    float h0, h1, h2, h3;
    {
      v4u a[4];
      const int base = WS_AP + (bpR * 32 + cgR) * 4096 + lane * 16;
      for (unsigned sn = 0; sn < SPIN_MAX; ++sn) {
        FENCE();
        bool ok = true;
#pragma unroll
        for (int i = 0; i < 4; ++i) a[i] = __builtin_amdgcn_raw_buffer_load_b128(rs, base + i * 1024, 0, AUX_SC1);
#pragma unroll
        for (int i = 0; i < 4; ++i) ok &= tag_ok(a[i], tag);
        if (__all(ok)) break;
      }
      STAMP(9);
      float s[4] = {(dec(a[0].x) + dec(a[1].x)) + (dec(a[2].x) + dec(a[3].x)), (dec(a[0].y) + dec(a[1].y)) + (dec(a[2].y) + dec(a[3].y)),
                    (dec(a[0].z) + dec(a[1].z)) + (dec(a[2].z) + dec(a[3].z)), (dec(a[0].w) + dec(a[1].w)) + (dec(a[2].w) + dec(a[3].w))};
#pragma unroll
      for (int i = 0; i < 4; ++i) { s[i] += dpp<0x128>(s[i]); s[i] = x16_sum(s[i]); s[i] = x32_sum(s[i]); }
      h0 = rb(lo_f(xres.x) + rb(s[0])); h1 = rb(hi_f(xres.x) + rb(s[1])); h2 = rb(lo_f(xres.y) + rb(s[2])); h3 = rb(hi_f(xres.y) + rb(s[3]));
      v4u o = {__float_as_uint(h0) | tag, __float_as_uint(h1) | tag, __float_as_uint(h2) | tag, __float_as_uint(h3) | tag};
      if (lane < 8) __builtin_amdgcn_raw_buffer_store_b128(o, rs, WS_HH + (bpR * 32 + cgR) * 128 + lane * 16, 0, AUX_SC1);
    }
    STAMP(10);
    {
      v4u a[4];
      const int base = WS_FP + (bpR * 32 + cgR) * (28 * 128);
      const int o3 = (lane < 32 ? 192 + lane : 128 + lane) * 16;
      for (unsigned sn = 0; sn < SPIN_MAX; ++sn) {
        FENCE();
        bool ok = true;
#pragma unroll
        for (int i = 0; i < 3; ++i) a[i] = __builtin_amdgcn_raw_buffer_load_b128(rs, base + i * 1024 + lane * 16, 0, AUX_SC1);
        a[3] = __builtin_amdgcn_raw_buffer_load_b128(rs, base + o3, 0, AUX_SC1);
#pragma unroll
        for (int i = 0; i < 4; ++i) ok &= tag_ok(a[i], tag);
        if (__all(ok)) break;
      }
      STAMP(11);
      const float k3 = lane < 32 ? 1.0f : 0.0f;
      float s[4] = {(dec(a[0].x) + dec(a[1].x)) + (dec(a[2].x) + k3 * dec(a[3].x)), (dec(a[0].y) + dec(a[1].y)) + (dec(a[2].y) + k3 * dec(a[3].y)),
                    (dec(a[0].z) + dec(a[1].z)) + (dec(a[2].z) + k3 * dec(a[3].z)), (dec(a[0].w) + dec(a[1].w)) + (dec(a[2].w) + k3 * dec(a[3].w))};
#pragma unroll
      for (int i = 0; i < 4; ++i) { s[i] += dpp<0x128>(s[i]); s[i] = x16_sum(s[i]); s[i] = x32_sum(s[i]); }
      float o0 = h0 + rb(rb(s[0]) + lo_f(b2lo)), o1 = h1 + rb(rb(s[1]) + hi_f(b2lo)), o2 = h2 + rb(rb(s[2]) + lo_f(b2hi)), o3f = h3 + rb(rb(s[3]) + hi_f(b2hi));
      if (lane < 8) *(v2u*)(out + (2 * bpR + (lane >> 2)) * 512 + 16 * cgR + 4 * (lane & 3)) = v2u{pk2(o0, o1), pk2(o2, o3f)};
      if (c == 0 && lane == 0) __builtin_amdgcn_raw_buffer_store_b32(E + 1, rs, WS_EP, 0, AUX_SC1);
    }
    STAMP(12);
    FLUSH();
    return;
  }
  if (w == 9) {
    __builtin_amdgcn_s_setprio(2);
    v4u xr[8], xs[2];
    const u16* xp = x + (lane >> 3) * 512 + (lane & 7) * 8;
#pragma unroll
    for (int i = 0; i < 8; ++i) xr[i] = *(const v4u*)(xp + 64 * i);
#pragma unroll
    for (int i = 0; i < 2; ++i) xs[i] = *(const v4u*)(xp + 128 * (r & 3) + 64 * i);
    BAR();
    {
      v2f f[32], s2 = {0, 0};
#pragma unroll
      for (int i = 0; i < 32; ++i) { f[i] = v2f{lo_f(xr[i >> 2][i & 3]), hi_f(xr[i >> 2][i & 3])}; s2 += f[i]; }
      float mean = sum8(s2.x + s2.y) * (1.0f / 512.0f);
      v2f q2 = {0, 0}, mm = {mean, mean};
#pragma unroll
      for (int i = 0; i < 32; ++i) { v2f d = f[i] - mm; q2 = __builtin_elementwise_fma(d, d, q2); }
      float var = sum8(q2.x + q2.y) * (1.0f / 512.0f);
      float mb = rb(mean), den = rb(sqrtf(rb(rb(var) + 1e-5f)));
      float inv = 1.0f / den;
#pragma unroll
      for (int i = 0; i < 2; ++i) {
        v4u o;
#pragma unroll
        for (int j = 0; j < 4; ++j) o[j] = pk2(rb(lo_f(xs[i][j]) - mb) * inv, rb(hi_f(xs[i][j]) - mb) * inv);
        *(LAS v4u*)(sm + L_XLN + (lane >> 3) * 256 + (64 * i + (lane & 7) * 8) * 2) = o;
      }
      lds_inc(flg + 0, lane);
    }
    STAMP(1);
    __builtin_amdgcn_s_setprio(0);
    if (isR) {
      lds_wait(flg + 3, 8);
      const int qp = r >> 2, cg = r & 3;
      const char* src = (const char*)wo + (size_t)(512 * xc + 128 * g + 32 * qp) * 1024 + cg * 256;
      const int pos = lane & 15;
#pragma unroll 4
      for (int j = 0; j < 32; ++j) {
        int f8 = g | (((j >> 1) & 1) << 2);
        glds_nt(src + (size_t)(8 * (j >> 3) + 2 * ((j >> 1) & 3) + (j & 1)) * 1024 + ((((pos >> 1) ^ f8) << 5) | ((pos & 1) << 4)), lds0 + L_K + j * 1024);
      }
      STAMP(2);
      BAR();
      VMCNT(0);
      STAMP(3);
      BAR();
    } else {
      BAR();
    }
    FLUSH();
    return;
  }
  v2u b1v = {0, 0};
  if (isM && w < 4) b1v = *(const v2u*)(b1 + 64 * hgM + 16 * w + 4 * g);
  BAR();
  __builtin_amdgcn_s_setprio(1);
  {
    const char* kb = (const char*)kc + ((size_t)(bq * 8 + xc) * 4096 + 3072 + sp * 256 + 32 * w) * 256;
#pragma unroll
    for (int i = 0; i < 8; ++i) {
      int key = 4 * i + g;
      glds_nt(kb + key * 256 + (((lane & 15) ^ (key & 15)) << 4), lds0 + L_K + (32 * w + 4 * i) * 256);
    }
  }
  __builtin_amdgcn_s_setprio(0);
  {
    const char* vb = (const char*)vc + ((size_t)(bq * 8 + xc) * 4096 + 3072 + sp * 256 + 32 * w) * 256;
    const int pos = lane & 15;
    const int o0 = g * 256 + ((((pos >> 1) ^ g) << 5) | ((pos & 1) << 4));
    const int o1 = g * 256 + ((((pos >> 1) ^ (4 + g)) << 5) | ((pos & 1) << 4));
#pragma unroll
    for (int j = 0; j < 8; ++j) glds_nt(vb + j * 1024 + ((j & 1) ? o1 : o0), lds0 + L_V + (8 * w + j) * 1024);
  }
  VMCNT(8);
  STAMP(1);
  lds_wait(flg + 1, 1);
  STAMP(2);
  __builtin_amdgcn_s_setprio(2);
  {
    v4f acc[2] = {v4f{0, 0, 0, 0}, v4f{0, 0, 0, 0}};
#pragma unroll
    for (int st = 0; st < 4; ++st) {
      v4u qf = *(LAS v4u*)(sm + L_QL + (cl & 3) * 256 + st * 64 + g * 16);
#pragma unroll
      for (int tt = 0; tt < 2; ++tt) {
        v4u kf = *(LAS v4u*)(sm + L_K + (32 * w + 16 * tt + cl) * 256 + (((4 * st + g) ^ cl) << 4));
        acc[tt] = mfma(kf, qf, acc[tt]);
      }
    }
    float s[8];
#pragma unroll
    for (int i = 0; i < 8; ++i) s[i] = rb(rb(acc[i >> 2][i & 3]) * 0.08838834764831845f);
    float mx = fmaxf(fmaxf(fmaxf(s[0], s[1]), fmaxf(s[2], s[3])), fmaxf(fmaxf(s[4], s[5]), fmaxf(s[6], s[7])));
    mx = x32_max(x16_max(mx));
    if (lane < 4) *(LAS float*)(sm + L_RMAX + lane * 32 + w * 4) = mx;
    lds_inc(flg + 2, lane);
    lds_wait(flg + 2, 8);
    v4f m0 = *(LAS v4f*)(sm + L_RMAX + (cl & 3) * 32), m1 = *(LAS v4f*)(sm + L_RMAX + (cl & 3) * 32 + 16);
    float m = fmaxf(fmaxf(fmaxf(m0.x, m0.y), fmaxf(m0.z, m0.w)), fmaxf(fmaxf(m1.x, m1.y), fmaxf(m1.z, m1.w)));
    float ls = 0;
#pragma unroll
    for (int i = 0; i < 8; ++i) { s[i] = __builtin_amdgcn_exp2f((s[i] - m) * 1.4426950408889634f); ls += s[i]; }
    ls = x32_sum(x16_sum(ls));
    if (lane < 4) *(LAS float*)(sm + L_RSUM + lane * 32 + w * 4) = ls;
    *(LAS v4u*)(sm + L_PF + w * 1024 + lane * 16) = v4u{pk2(s[0], s[1]), pk2(s[2], s[3]), pk2(s[4], s[5]), pk2(s[6], s[7])};
    lds_inc(flg + 3, lane);
  }
  STAMP(3);
  lds_wait(flg + 3, 8);
  v4u pf[8];
#pragma unroll
  for (int m = 0; m < 8; ++m) pf[m] = *(LAS v4u*)(sm + L_PF + m * 1024 + lane * 16);
  VMCNT(0);
  STAMP(4);
  BAR();
  tag = flg[4] & 0xff;
  {
    LAS char* p = sm + L_V + (4 * g + tq) * 256 + ((w ^ (4 * (g & 1) + tq)) << 5) + tp * 8;
    v4f acc = {0, 0, 0, 0};
#pragma unroll
    for (int m = 0; m < 8; ++m) acc = mfma(pf[m], trfrag(p + m * 8192, 4096), acc);
    v4u o = {enc(acc.x, tag), enc(acc.y, tag), enc(acc.z, tag), enc(acc.w, tag)};
    if (lane < 16)
      __builtin_amdgcn_raw_buffer_store_b128(o, rs, WS_OP + (((xc * 4 + (w >> 1)) * 8 + bq) * 4 + sp) * 512 + ((w & 1) * 16 + lane) * 16, 0, AUX_SC1);
  }
  STAMP(6);
  const int f8 = (tq & 3) | ((g & 1) << 2);
  if (isR) {
    BAR();
    v4u wof[4];
    {
      LAS char* p = sm + L_K + (8 * g + tq) * 256 + ((w ^ f8) << 5) + tp * 8;
#pragma unroll
      for (int m = 0; m < 4; ++m) wof[m] = trfrag(p + m * 8192, 1024);
    }
    {
      v4u a[2], mu, lu;
      const int base = WS_OP + ((xc * 4 + (r >> 2)) * 8 + w) * 2048 + lane * 16;
      const int sb = WS_ST + (xc * 8 + w) * 512 + (lane & 3) * 128;
      for (unsigned sn = 0; sn < SPIN_MAX; ++sn) {
        FENCE();
        a[0] = __builtin_amdgcn_raw_buffer_load_b128(rs, base, 0, AUX_SC1);
        a[1] = __builtin_amdgcn_raw_buffer_load_b128(rs, base + 1024, 0, AUX_SC1);
        mu = __builtin_amdgcn_raw_buffer_load_b128(rs, sb, 0, AUX_SC1);
        lu = __builtin_amdgcn_raw_buffer_load_b128(rs, sb + 16, 0, AUX_SC1);
        if (__all(tag_ok(a[0], tag) && tag_ok(a[1], tag) && tag_ok(mu, tag) && tag_ok(lu, tag))) break;
      }
      STAMP(7);
      float ov[4];
#pragma unroll
      for (int i = 0; i < 4; ++i) {
        float mi = dec(mu[i]), li = dec(lu[i]);
        float M = fmaxf(mi, dpp<0xB1>(mi)); M = fmaxf(M, dpp<0x4E>(M));
        float sc = __builtin_amdgcn_exp2f((mi - M) * 1.4426950408889634f);
        float L = li * sc; L += dpp<0xB1>(L); L += dpp<0x4E>(L);
        float fi = sc * __builtin_amdgcn_rcpf(L);
        float f0 = dpp<0x00>(fi), f1 = dpp<0x55>(fi), f2 = dpp<0xAA>(fi), f3 = dpp<0xFF>(fi);
        float fa = lane < 32 ? f0 : f1, fb = lane < 32 ? f2 : f3;
        ov[i] = x32_sum(dec(a[0][i]) * fa + dec(a[1][i]) * fb);
      }
      if (lane < 32) *(LAS v2u*)(sm + L_OB + (lane >> 3) * 512 + ((lane & 7) >> 1) * 128 + w * 16 + (lane & 1) * 8) = v2u{pk2(ov[0], ov[1]), pk2(ov[2], ov[3])};
    }
    BAR();
    {
      v4f acc = {0, 0, 0, 0};
#pragma unroll
      for (int m = 0; m < 4; ++m) acc = mfma(wof[m], *(LAS v4u*)(sm + L_OB + m * 512 + g * 128 + (cl & 7) * 16), acc);
      const int src = 2 * (lane >> 3) + ((lane >> 2) & 1) + 16 * (lane & 3);
      v4u o = {enc(bperm(src, acc.x), tag), enc(bperm(src, acc.y), tag), enc(bperm(src, acc.z), tag), enc(bperm(src, acc.w), tag)};
      if (lane < 32)
        __builtin_amdgcn_raw_buffer_store_b128(o, rs, WS_AP + (((lane >> 3) * 32 + 8 * (r & 3) + w) * 32 + (xc * 4 + (r >> 2))) * 128 + (lane & 7) * 16, 0, AUX_SC1);
    }
    STAMP(8);
  } else if (isM) {
    {
      const char* src = (const char*)w1 + (size_t)(64 * w + (lane >> 3)) * 3584 + hgM * 128;
      const int s0 = ((lane & 7) ^ (((lane >> 4) & 1) << 1)) << 4;
#pragma unroll
      for (int j = 0; j < 8; ++j) glds(src + (size_t)j * 8 * 3584 + (s0 ^ ((j & 1) << 6)), lds0 + L_K + (8 * w + j) * 1024);
    }
    BAR();
    {
      const char* src = (const char*)w2 + (size_t)(64 * hgM + 32 * (w >> 2) + 4 * (w & 3)) * 1024;
#pragma unroll
      for (int j = 0; j < 8; ++j) {
        int ff = (j & 3) | ((w & 1) << 2);
        glds(src + (size_t)(16 * (j >> 2) + (j & 3)) * 1024 + ((((lane >> 1) ^ ff) << 5) | ((lane & 1) << 4)), lds0 + L_V + (8 * w + j) * 1024);
      }
    }
    VMCNT(0);
    STAMP(9);
    BAR();
    v4u bf[8];
    {
      LAS char* p = sm + L_V + (8 * g + tq) * 1024 + tp * 8;
#pragma unroll
      for (int j = 0; j < 4; ++j)
#pragma unroll
        for (int m = 0; m < 2; ++m) bf[2 * j + m] = trfrag(p + (((4 * w + j) ^ f8) << 5) + m * 32768, 4096);
    }
    if (w < 4) {
      v4u af[16];
      const int wf = ((tq >> 1) & 1) | ((g & 1) << 1);
      LAS char* p = sm + L_K + (8 * g + tq) * 128 + tp * 8 + ((w ^ wf) << 5);
#pragma unroll
      for (int st = 0; st < 16; ++st) af[st] = trfrag(p + st * 4096, 512);
      BAR();
      STAMP(11);
      v4f acc = {0, 0, 0, 0};
#pragma unroll
      for (int st = 0; st < 16; ++st) acc = mfma(af[st], *(LAS v4u*)(sm + L_Y + (cl & 1) * 1024 + st * 64 + g * 16), acc);
      float h0 = gelu_t(rb(rb(acc.x) + lo_f(b1v.x))), h1 = gelu_t(rb(rb(acc.y) + hi_f(b1v.x)));
      float h2 = gelu_t(rb(rb(acc.z) + lo_f(b1v.y))), h3 = gelu_t(rb(rb(acc.w) + hi_f(b1v.y)));
      if (cl < 2) *(LAS v2u*)(sm + L_HB + (w >> 1) * 128 + g * 32 + cl * 16 + (w & 1) * 8) = v2u{pk2(h0, h1), pk2(h2, h3)};
    } else if (w < 6) {
      const int bl = w - 4;
      v4u a[2];
      const int base = WS_HH + bpM * 4096 + (lane >> 2) * 128 + bl * 64 + (lane & 3) * 16;
      for (unsigned sn = 0; sn < SPIN_MAX; ++sn) {
        FENCE();
        a[0] = __builtin_amdgcn_raw_buffer_load_b128(rs, base, 0, AUX_SC1);
        a[1] = __builtin_amdgcn_raw_buffer_load_b128(rs, base + 2048, 0, AUX_SC1);
        if (__all(tag_ok(a[0], tag) && tag_ok(a[1], tag))) break;
      }
      STAMP(10);
      float v[8] = {dec(a[0].x), dec(a[0].y), dec(a[0].z), dec(a[0].w), dec(a[1].x), dec(a[1].y), dec(a[1].z), dec(a[1].w)};
#pragma unroll
      for (int pass = 0; pass < 2; ++pass) {
        float s = ((v[0] + v[1]) + (v[2] + v[3])) + ((v[4] + v[5]) + (v[6] + v[7]));
        float mean = sum64(s) * (1.0f / 512.0f);
        float q = 0;
#pragma unroll
        for (int i = 0; i < 8; ++i) { float d = v[i] - mean; q += d * d; }
        float var = sum64(q) * (1.0f / 512.0f);
        float mb = rb(mean), inv = 1.0f / rb(sqrtf(rb(rb(var) + 1e-5f)));
#pragma unroll
        for (int i = 0; i < 8; ++i) v[i] = rb(rb(v[i] - mb) * inv);
      }
      *(LAS v2u*)(sm + L_Y + bl * 1024 + lane * 8) = v2u{pk2(v[0], v[1]), pk2(v[2], v[3])};
      *(LAS v2u*)(sm + L_Y + bl * 1024 + 512 + lane * 8) = v2u{pk2(v[4], v[5]), pk2(v[6], v[7])};
      BAR();
    } else {
      BAR();
    }
    BAR();
    {
      v4u hb0 = *(LAS v4u*)(sm + L_HB + g * 32 + (cl & 1) * 16), hb1 = *(LAS v4u*)(sm + L_HB + 128 + g * 32 + (cl & 1) * 16);
      v4f acc[4];
#pragma unroll
      for (int j = 0; j < 4; ++j) { acc[j] = mfma(bf[2 * j], hb0, v4f{0, 0, 0, 0}); acc[j] = mfma(bf[2 * j + 1], hb1, acc[j]); }
      if (cl < 2) {
#pragma unroll
        for (int j = 0; j < 4; ++j) *(LAS v4f*)(sm + L_TB + w * 512 + j * 128 + cl * 64 + g * 16) = acc[j];
      }
      FENCE();
      v4f v = *(LAS v4f*)(sm + L_TB + w * 512 + (lane & 31) * 16);
      v4u o = {enc(v.x, tag), enc(v.y, tag), enc(v.z, tag), enc(v.w, tag)};
      if (lane < 32)
        __builtin_amdgcn_raw_buffer_store_b128(o, rs, WS_FP + ((bpM * 32 + 4 * w + (lane >> 3)) * 28 + hgM) * 128 + (lane & 7) * 16, 0, AUX_SC1);
    }
    STAMP(12);
  }
  FLUSH();
}

extern "C" void kernel_launch(void* const* d_in, const int* in_sizes, int n_in, void* d_out, int out_size, void* d_ws, size_t ws_size, hipStream_t stream) {
  fused_decode_kernel<<<256, 640, 0, stream>>>((const u16*)d_in[0], (const u16*)d_in[1], (const u16*)d_in[2], (const u16*)d_in[3], (const u16*)d_in[4],
                                               (const u16*)d_in[6], (const u16*)d_in[7], (const u16*)d_in[8], (const u16*)d_in[9], (u16*)d_out, (char*)d_ws);
}
```

```cpp
#include <hip/hip_runtime.h>
#include <stdint.h>

typedef unsigned short u16;
typedef unsigned v4u __attribute__((ext_vector_type(4)));
typedef unsigned v2u __attribute__((ext_vector_type(2)));
typedef float v4f __attribute__((ext_vector_type(4)));
typedef short v4s __attribute__((ext_vector_type(4)));
typedef __bf16 v8b __attribute__((ext_vector_type(8)));
#define LAS __attribute__((address_space(3)))
#define DEV __device__ __forceinline__

constexpr int WS_EP = 0;
constexpr int WS_ST = 4096;
constexpr int WS_HH = WS_ST + 32768;
constexpr int WS_QP = WS_HH + 16384;
constexpr int WS_OP = WS_QP + 524288;
constexpr int WS_AP = WS_OP + 524288;
constexpr int WS_FP = WS_AP + 524288;
constexpr int WS_END = WS_FP + 128 * 28 * 128;
constexpr int L_K = 0, L_V = 65536, L_W = 131072, L_M = 147456;
constexpr int L_XLN = L_M, L_OB = L_M, L_QL = L_M + 2048, L_PF = L_M + 3072, L_TQ = L_PF, L_Y = L_PF, L_TB = L_PF + 2048, L_HB = L_M + 2048;
constexpr int L_RMAX = L_M + 11264, L_RSUM = L_M + 11392, L_FLG = L_M + 11552;
constexpr int LDS_BYTES = 163840;
constexpr int AUX_SC1 = 16;
constexpr unsigned SPIN_MAX = 200000u;

#define STAMP(i) do {} while (0)
#define FLUSH() do {} while (0)

DEV float bfu(unsigned hi16) { return __uint_as_float(hi16 << 16); }
DEV float lo_f(unsigned p) { return __uint_as_float(p << 16); }
DEV float hi_f(unsigned p) { return __uint_as_float(p & 0xffff0000u); }
typedef __bf16 v2b __attribute__((ext_vector_type(2)));
typedef float v2f __attribute__((ext_vector_type(2)));
DEV unsigned pk2(float lo, float hi) { return __builtin_bit_cast(unsigned, __builtin_convertvector(v2f{lo, hi}, v2b)); }
DEV float rb(float f) { return lo_f(pk2(f, f)); }
DEV unsigned enc(float v, unsigned t) { return ((__float_as_uint(v) + 0x80u) & 0xffffff00u) | t; }
DEV float dec(unsigned u) { return __uint_as_float(u & 0xffffff00u); }
DEV bool tag_ok(v4u x, unsigned t) { return ((((x.x ^ t) | (x.y ^ t)) | ((x.z ^ t) | (x.w ^ t))) & 0xffu) == 0; }
template <int C> DEV float dpp(float x) { return __builtin_bit_cast(float, __builtin_amdgcn_update_dpp(0, __builtin_bit_cast(int, x), C, 0xf, 0xf, true)); }
DEV float x16_sum(float x) { auto s = __builtin_amdgcn_permlane16_swap(__float_as_uint(x), __float_as_uint(x), false, false); return __uint_as_float(s[0]) + __uint_as_float(s[1]); }
DEV float x32_sum(float x) { auto s = __builtin_amdgcn_permlane32_swap(__float_as_uint(x), __float_as_uint(x), false, false); return __uint_as_float(s[0]) + __uint_as_float(s[1]); }
DEV float x16_max(float x) { auto s = __builtin_amdgcn_permlane16_swap(__float_as_uint(x), __float_as_uint(x), false, false); return fmaxf(__uint_as_float(s[0]), __uint_as_float(s[1])); }
DEV float x32_max(float x) { auto s = __builtin_amdgcn_permlane32_swap(__float_as_uint(x), __float_as_uint(x), false, false); return fmaxf(__uint_as_float(s[0]), __uint_as_float(s[1])); }
DEV float sum8(float x) { x += dpp<0xB1>(x); x += dpp<0x4E>(x); x += dpp<0x141>(x); return x; }
DEV float sum16(float x) { x = sum8(x); x += dpp<0x140>(x); return x; }
DEV float fsqrt(float x) { return __builtin_amdgcn_sqrtf(x); }
DEV float frcp(float x) { return __builtin_amdgcn_rcpf(x); }
#define THIN_D 1
#define STR2(x) #x
#define STR(x) STR2(x)
DEV float sum64(float x) { x = sum8(x); x += dpp<0x140>(x); x = x16_sum(x); return x32_sum(x); }
DEV void glds_nt(const void* g, unsigned lds) {
  unsigned keep;
  asm volatile("s_mov_b32 %0, m0\n\ts_mov_b32 m0, %2\n\ts_nop 0\n\tglobal_load_lds_dwordx4 %1, off nt\n\ts_mov_b32 m0, %0" : "=&s"(keep) : "v"(g), "s"(lds) : "memory");
}
DEV void glds(const void* g, unsigned lds) {
  unsigned keep;
  asm volatile("s_mov_b32 %0, m0\n\ts_mov_b32 m0, %2\n\ts_nop 0\n\tglobal_load_lds_dwordx4 %1, off\n\ts_mov_b32 m0, %0" : "=&s"(keep) : "v"(g), "s"(lds) : "memory");
}
#define BAR() asm volatile("s_waitcnt lgkmcnt(0)\n\ts_barrier" ::: "memory")
#define VMCNT(n) asm volatile("s_waitcnt vmcnt(" #n ")" ::: "memory")
#define FENCE() asm volatile("" ::: "memory")
DEV v8b mk(v4u a) { return __builtin_bit_cast(v8b, a); }
DEV v4f mfma(v4u a, v4u b, v4f c) { return __builtin_amdgcn_mfma_f32_16x16x32_bf16(mk(a), mk(b), c, 0, 0, 0); }
DEV v4u trfrag(LAS char* p, int off2) {
  v4s a = __builtin_amdgcn_ds_read_tr16_b64_v4i16((LAS v4s*)p);
  v4s b = __builtin_amdgcn_ds_read_tr16_b64_v4i16((LAS v4s*)(p + off2));
  v2u a2 = __builtin_bit_cast(v2u, a), b2 = __builtin_bit_cast(v2u, b);
  return v4u{a2.x, a2.y, b2.x, b2.y};
}
DEV void lds_wait(LAS unsigned* p, unsigned v) {
  for (unsigned s = 0; s < 50000000u; ++s) { if (*(volatile LAS unsigned*)p >= v) break; }
  FENCE();
}
DEV void lds_inc(LAS unsigned* p, int lane) {
  asm volatile("s_waitcnt lgkmcnt(0)" ::: "memory");
  if (lane == 0) __hip_atomic_fetch_add(p, 1u, __ATOMIC_RELAXED, __HIP_MEMORY_SCOPE_WORKGROUP);
  FENCE();
}
DEV float bperm(int srclane, float v) { return __builtin_bit_cast(float, __builtin_amdgcn_ds_bpermute(srclane << 2, __builtin_bit_cast(int, v))); }
DEV float gelu_t(float z) {
  float u = 0.7978845608028654f * (z + 0.044715f * z * z * z);
  float e = __builtin_amdgcn_exp2f(-2.885390081777927f * u);
  return z * __builtin_amdgcn_rcpf(1.0f + e);
}

__global__ __launch_bounds__(704) void fused_decode_kernel(const u16* __restrict__ x, const u16* __restrict__ kc, const u16* __restrict__ vc,
    const u16* __restrict__ wq, const u16* __restrict__ wo, const u16* __restrict__ w1, const u16* __restrict__ b1, const u16* __restrict__ w2,
    const u16* __restrict__ b2, u16* __restrict__ out, char* ws
    ) {
  __shared__ __attribute__((aligned(1024))) char smem_[LDS_BYTES];
  LAS char* sm = (LAS char*)smem_;
  const unsigned lds0 = (unsigned)(size_t)sm;
  const int lane = threadIdx.x & 63, w = __builtin_amdgcn_readfirstlane(threadIdx.x >> 6);
  const int c = blockIdx.x, xc = c & 7, r = c >> 3;
  const int bq = r >> 2, sp = r & 3;
  const int cl = lane & 15, g = lane >> 4;
  __amdgpu_buffer_rsrc_t rs = __builtin_amdgcn_make_buffer_rsrc(ws, 0, WS_END, 0x00020000);
  LAS unsigned* flg = (LAS unsigned*)(sm + L_FLG);
  STAMP(0);
  const bool isR = r < 16, isM = (r >= 16) && (r < 30);
  const int bpR = r >> 2, cgR = 4 * xc + (r & 3);
  const int im = 14 * xc + (r - 16), hgM = im >> 2, bpM = im & 3;
  const int tq = (lane >> 2) & 3, tp = lane & 3;
  unsigned tag = 0;

  if (w == 8) {
    __builtin_amdgcn_s_setprio(3);
    unsigned E = __builtin_amdgcn_raw_buffer_load_b32(rs, WS_EP, 0, AUX_SC1);
    {
      const int kq = r & 3, cg = r >> 2;
      const char* src = (const char*)wq + (size_t)(128 * kq + (lane >> 3)) * 8192 + (512 * xc + 64 * cg) * 2;
      const int s0 = ((lane & 7) ^ (((lane >> 4) & 1) << 1)) << 4;
#pragma unroll
      for (int j = 0; j < 16; ++j) glds_nt(src + (size_t)j * 8 * 8192 + (s0 ^ ((j & 1) << 6)), lds0 + L_W + j * 1024);
    }
    if (lane < 8) flg[lane] = 0;
    BAR();
    VMCNT(0);
    STAMP(1);
    E = __builtin_amdgcn_readfirstlane(E);
    tag = (E + 1) & 0xff;
    if (lane == 0) flg[4] = tag | 0x100;
    {
      v4u wa[16];
      const int wf = ((tq >> 1) & 1) | ((g & 1) << 1);
#pragma unroll
      for (int t = 0; t < 4; ++t) {
        LAS char* p = sm + L_W + (8 * g + tq) * 128 + tp * 8 + ((t ^ wf) << 5);
#pragma unroll
        for (int st = 0; st < 4; ++st) wa[4 * t + st] = trfrag(p + st * 4096, 512);
      }
      lds_wait(flg + 0, 2);
      v4u xb[4];
#pragma unroll
      for (int st = 0; st < 4; ++st) xb[st] = *(LAS v4u*)(sm + L_XLN + (cl & 7) * 256 + st * 64 + g * 16);
      FENCE();
      v4f acc[4] = {v4f{0, 0, 0, 0}, v4f{0, 0, 0, 0}, v4f{0, 0, 0, 0}, v4f{0, 0, 0, 0}};
#pragma unroll
      for (int st = 0; st < 4; ++st)
#pragma unroll
        for (int t = 0; t < 4; ++t) acc[t] = mfma(wa[4 * t + st], xb[st], acc[t]);
      if (cl < 8) {
#pragma unroll
        for (int t = 0; t < 4; ++t) *(LAS v4f*)(sm + L_TQ + cl * 272 + (16 * t + 4 * g) * 4) = acc[t];
      }
      FENCE();
      const int kq = r & 3, cg = r >> 2;
#pragma unroll
      for (int i = 0; i < 2; ++i) {
        int idx = 64 * i + lane, b = idx >> 4, u = idx & 15;
        v4f v = *(LAS v4f*)(sm + L_TQ + b * 272 + u * 16);
        v4u o = {enc(v.x, tag), enc(v.y, tag), enc(v.z, tag), enc(v.w, tag)};
        __builtin_amdgcn_raw_buffer_store_b128(o, rs, WS_QP + ((xc * 8 + b) * 4 + kq) * 2048 + cg * 256 + u * 16, 0, AUX_SC1);
      }
    }
    STAMP(2);
    {
      v4u q[8];
      const int base = WS_QP + (xc * 8 + bq) * 8192 + lane * 16;
      for (unsigned sn = 0; sn < SPIN_MAX; ++sn) {
        FENCE();
        bool ok = true;
#pragma unroll
        for (int i = 0; i < 8; ++i) q[i] = __builtin_amdgcn_raw_buffer_load_b128(rs, base + i * 1024, 0, AUX_SC1);
#pragma unroll
        for (int i = 0; i < 8; ++i) ok &= tag_ok(q[i], tag);
        if (__all(ok)) break;
      }
#pragma unroll
      for (int hf = 0; hf < 2; ++hf) {
        float a0 = (dec(q[hf].x) + dec(q[2 + hf].x)) + (dec(q[4 + hf].x) + dec(q[6 + hf].x));
        float a1 = (dec(q[hf].y) + dec(q[2 + hf].y)) + (dec(q[4 + hf].y) + dec(q[6 + hf].y));
        float a2 = (dec(q[hf].z) + dec(q[2 + hf].z)) + (dec(q[4 + hf].z) + dec(q[6 + hf].z));
        float a3 = (dec(q[hf].w) + dec(q[2 + hf].w)) + (dec(q[4 + hf].w) + dec(q[6 + hf].w));
        *(LAS v2u*)(sm + L_QL + (hf * 256 + 4 * lane) * 2) = v2u{pk2(a0, a1), pk2(a2, a3)};
      }
      lds_inc(flg + 1, lane);
    }
    STAMP(3);
    v2u xres = {0, 0};
    unsigned b2lo = 0, b2hi = 0;
    if (isR) {
      xres = *(const v2u*)(x + (2 * bpR + ((lane >> 2) & 1)) * 512 + 16 * cgR + 4 * (lane & 3));
      v2u bb = *(const v2u*)(b2 + 16 * cgR + 4 * (lane & 3));
      b2lo = bb.x; b2hi = bb.y;
    }
    BAR();
    BAR();
    {
      const int h = lane & 3;
      v4f m0 = *(LAS v4f*)(sm + L_RMAX + h * 32), m1 = *(LAS v4f*)(sm + L_RMAX + h * 32 + 16);
      v4f s0 = *(LAS v4f*)(sm + L_RSUM + h * 32), s1 = *(LAS v4f*)(sm + L_RSUM + h * 32 + 16);
      float m = fmaxf(fmaxf(fmaxf(m0.x, m0.y), fmaxf(m0.z, m0.w)), fmaxf(fmaxf(m1.x, m1.y), fmaxf(m1.z, m1.w)));
      float l = ((s0.x + s0.y) + (s0.z + s0.w)) + ((s1.x + s1.y) + (s1.z + s1.w));
      float v = (lane & 4) ? l : m;
      v4u o = {enc(dpp<0x00>(v), tag), enc(dpp<0x55>(v), tag), enc(dpp<0xAA>(v), tag), enc(dpp<0xFF>(v), tag)};
      if (lane == 0 || lane == 4) __builtin_amdgcn_raw_buffer_store_b128(o, rs, WS_ST + (xc * 8 + bq) * 512 + sp * 128 + (lane >> 2) * 16, 0, AUX_SC1);
    }
    STAMP(4);
    BAR();
    if (isM) { BAR(); BAR(); BAR(); BAR(); FLUSH(); return; }
    if (!isR) { FLUSH(); return; }
    BAR();
    BAR();
    float h0, h1, h2, h3;
    {
      v4u a[4];
      const int base = WS_AP + (bpR * 32 + cgR) * 4096 + lane * 16;
      for (unsigned sn = 0; sn < SPIN_MAX; ++sn) {
        FENCE();
        bool ok = true;
#pragma unroll
        for (int i = 0; i < 4; ++i) a[i] = __builtin_amdgcn_raw_buffer_load_b128(rs, base + i * 1024, 0, AUX_SC1);
#pragma unroll
        for (int i = 0; i < 4; ++i) ok &= tag_ok(a[i], tag);
        if (__all(ok)) break;
      }
      STAMP(9);
      float s[4] = {(dec(a[0].x) + dec(a[1].x)) + (dec(a[2].x) + dec(a[3].x)), (dec(a[0].y) + dec(a[1].y)) + (dec(a[2].y) + dec(a[3].y)),
                    (dec(a[0].z) + dec(a[1].z)) + (dec(a[2].z) + dec(a[3].z)), (dec(a[0].w) + dec(a[1].w)) + (dec(a[2].w) + dec(a[3].w))};
#pragma unroll
      for (int i = 0; i < 4; ++i) { s[i] += dpp<0x128>(s[i]); s[i] = x16_sum(s[i]); s[i] = x32_sum(s[i]); }
      h0 = rb(lo_f(xres.x) + rb(s[0])); h1 = rb(hi_f(xres.x) + rb(s[1])); h2 = rb(lo_f(xres.y) + rb(s[2])); h3 = rb(hi_f(xres.y) + rb(s[3]));
      v4u o = {__float_as_uint(h0) | tag, __float_as_uint(h1) | tag, __float_as_uint(h2) | tag, __float_as_uint(h3) | tag};
      if (lane < 8) __builtin_amdgcn_raw_buffer_store_b128(o, rs, WS_HH + (bpR * 32 + cgR) * 128 + lane * 16, 0, AUX_SC1);
    }
    STAMP(10);
    {
      v4u a[4];
      const int base = WS_FP + (bpR * 32 + cgR) * (28 * 128);
      const int o3 = (lane < 32 ? 192 + lane : 128 + lane) * 16;
      for (unsigned sn = 0; sn < SPIN_MAX; ++sn) {
        FENCE();
        bool ok = true;
#pragma unroll
        for (int i = 0; i < 3; ++i) a[i] = __builtin_amdgcn_raw_buffer_load_b128(rs, base + i * 1024 + lane * 16, 0, AUX_SC1);
        a[3] = __builtin_amdgcn_raw_buffer_load_b128(rs, base + o3, 0, AUX_SC1);
#pragma unroll
        for (int i = 0; i < 4; ++i) ok &= tag_ok(a[i], tag);
        if (__all(ok)) break;
      }
      STAMP(11);
      const float k3 = lane < 32 ? 1.0f : 0.0f;
      float s[4] = {(dec(a[0].x) + dec(a[1].x)) + (dec(a[2].x) + k3 * dec(a[3].x)), (dec(a[0].y) + dec(a[1].y)) + (dec(a[2].y) + k3 * dec(a[3].y)),
                    (dec(a[0].z) + dec(a[1].z)) + (dec(a[2].z) + k3 * dec(a[3].z)), (dec(a[0].w) + dec(a[1].w)) + (dec(a[2].w) + k3 * dec(a[3].w))};
#pragma unroll
      for (int i = 0; i < 4; ++i) { s[i] += dpp<0x128>(s[i]); s[i] = x16_sum(s[i]); s[i] = x32_sum(s[i]); }
      float o0 = h0 + rb(rb(s[0]) + lo_f(b2lo)), o1 = h1 + rb(rb(s[1]) + hi_f(b2lo)), o2 = h2 + rb(rb(s[2]) + lo_f(b2hi)), o3f = h3 + rb(rb(s[3]) + hi_f(b2hi));
      if (lane < 8) *(v2u*)(out + (2 * bpR + (lane >> 2)) * 512 + 16 * cgR + 4 * (lane & 3)) = v2u{pk2(o0, o1), pk2(o2, o3f)};
      if (c == 0 && lane == 0) __builtin_amdgcn_raw_buffer_store_b32(E + 1, rs, WS_EP, 0, AUX_SC1);
    }
    STAMP(12);
    FLUSH();
    return;
  }
  if (w >= 9) {
    __builtin_amdgcn_s_setprio(2);
    v4u xr[4], xs;
    const int xb_ = 4 * (w - 9) + g;
    const u16* xp = x + xb_ * 512 + cl * 8;
#pragma unroll
    for (int i = 0; i < 4; ++i) xr[i] = *(const v4u*)(xp + 128 * i);
    xs = *(const v4u*)(xp + 128 * (r & 3));
    BAR();
    {
      v2f f[16], s2[4] = {v2f{0, 0}, v2f{0, 0}, v2f{0, 0}, v2f{0, 0}};
#pragma unroll
      for (int i = 0; i < 16; ++i) { f[i] = v2f{lo_f(xr[i >> 2][i & 3]), hi_f(xr[i >> 2][i & 3])}; s2[i & 3] += f[i]; }
      v2f st = (s2[0] + s2[1]) + (s2[2] + s2[3]);
      float mean = sum16(st.x + st.y) * (1.0f / 512.0f);
      v2f q2[4] = {v2f{0, 0}, v2f{0, 0}, v2f{0, 0}, v2f{0, 0}}, mm = {mean, mean};
#pragma unroll
      for (int i = 0; i < 16; ++i) { v2f d = f[i] - mm; q2[i & 3] = __builtin_elementwise_fma(d, d, q2[i & 3]); }
      v2f qt = (q2[0] + q2[1]) + (q2[2] + q2[3]);
      float var = sum16(qt.x + qt.y) * (1.0f / 512.0f);
      float mb = rb(mean), inv = frcp(rb(fsqrt(rb(rb(var) + 1e-5f))));
      v4u o;
#pragma unroll
      for (int j = 0; j < 4; ++j) o[j] = pk2(rb(lo_f(xs[j]) - mb) * inv, rb(hi_f(xs[j]) - mb) * inv);
      *(LAS v4u*)(sm + L_XLN + xb_ * 256 + cl * 16) = o;
      lds_inc(flg + 0, lane);
    }
    if (w == 10) { FLUSH(); return; }
    STAMP(1);
    __builtin_amdgcn_s_setprio(0);
    BAR();
    BAR();
    if (isR) {
      const int qp = r >> 2, cg = r & 3;
      const char* src = (const char*)wo + (size_t)(512 * xc + 128 * g + 32 * qp) * 1024 + cg * 256;
      const int pos = lane & 15;
#pragma unroll 4
      for (int j = 0; j < 32; ++j) {
        int f8 = g | (((j >> 1) & 1) << 2);
        glds_nt(src + (size_t)(8 * (j >> 3) + 2 * ((j >> 1) & 3) + (j & 1)) * 1024 + ((((pos >> 1) ^ f8) << 5) | ((pos & 1) << 4)), lds0 + L_K + j * 1024);
      }
      STAMP(2);
      BAR();
      VMCNT(0);
      STAMP(3);
      BAR();
    } else {
      BAR();
    }
    FLUSH();
    return;
  }
  v2u b1v = {0, 0};
  if (isM && w < 4) b1v = *(const v2u*)(b1 + 64 * hgM + 16 * w + 4 * g);
  BAR();
  __builtin_amdgcn_s_setprio(1);
  {
    bool full = false;
    const char* kb = (const char*)kc + ((size_t)(bq * 8 + xc) * 4096 + 3072 + sp * 256 + 32 * w) * 256;
#pragma unroll
    for (int i = 0; i < 8; ++i) {
      int key = 4 * i + g;
      glds_nt(kb + key * 256 + (((lane & 15) ^ (key & 15)) << 4), lds0 + L_K + (32 * w + 4 * i) * 256);
      if (THIN_D > 0 && i >= THIN_D && !full) { asm volatile("s_waitcnt vmcnt(" STR(THIN_D) ")" ::: "memory"); full = __builtin_amdgcn_readfirstlane(*(volatile LAS unsigned*)(flg + 1)) != 0; }
    }
    __builtin_amdgcn_s_setprio(0);
    const char* vb = (const char*)vc + ((size_t)(bq * 8 + xc) * 4096 + 3072 + sp * 256 + 32 * w) * 256;
    const int pos = lane & 15;
    const int o0 = g * 256 + ((((pos >> 1) ^ g) << 5) | ((pos & 1) << 4));
    const int o1 = g * 256 + ((((pos >> 1) ^ (4 + g)) << 5) | ((pos & 1) << 4));
#pragma unroll
    for (int j = 0; j < 8; ++j) {
      glds_nt(vb + j * 1024 + ((j & 1) ? o1 : o0), lds0 + L_V + (8 * w + j) * 1024);
      if (THIN_D > 0 && j < 7 && !full) { asm volatile("s_waitcnt vmcnt(" STR(THIN_D) ")" ::: "memory"); full = __builtin_amdgcn_readfirstlane(*(volatile LAS unsigned*)(flg + 1)) != 0; }
    }
  }
  VMCNT(8);
  STAMP(1);
  lds_wait(flg + 1, 1);
  STAMP(2);
  __builtin_amdgcn_s_setprio(2);
  {
    v4f acc[2] = {v4f{0, 0, 0, 0}, v4f{0, 0, 0, 0}};
    v4u qf[4], kf[8];
#pragma unroll
    for (int st = 0; st < 4; ++st) {
      qf[st] = *(LAS v4u*)(sm + L_QL + (cl & 3) * 256 + st * 64 + g * 16);
#pragma unroll
      for (int tt = 0; tt < 2; ++tt) kf[2 * st + tt] = *(LAS v4u*)(sm + L_K + (32 * w + 16 * tt + cl) * 256 + (((4 * st + g) ^ cl) << 4));
    }
    FENCE();
#pragma unroll
    for (int st = 0; st < 4; ++st)
#pragma unroll
      for (int tt = 0; tt < 2; ++tt) acc[tt] = mfma(kf[2 * st + tt], qf[st], acc[tt]);
    float s[8];
#pragma unroll
    for (int i = 0; i < 8; ++i) s[i] = rb(rb(acc[i >> 2][i & 3]) * 0.08838834764831845f);
    float mx = fmaxf(fmaxf(fmaxf(s[0], s[1]), fmaxf(s[2], s[3])), fmaxf(fmaxf(s[4], s[5]), fmaxf(s[6], s[7])));
    mx = x32_max(x16_max(mx));
    if (lane < 4) *(LAS float*)(sm + L_RMAX + lane * 32 + w * 4) = mx;
    BAR();
    v4f m0 = *(LAS v4f*)(sm + L_RMAX + (cl & 3) * 32), m1 = *(LAS v4f*)(sm + L_RMAX + (cl & 3) * 32 + 16);
    float m = fmaxf(fmaxf(fmaxf(m0.x, m0.y), fmaxf(m0.z, m0.w)), fmaxf(fmaxf(m1.x, m1.y), fmaxf(m1.z, m1.w)));
    float ls = 0;
#pragma unroll
    for (int i = 0; i < 8; ++i) { s[i] = __builtin_amdgcn_exp2f((s[i] - m) * 1.4426950408889634f); ls += s[i]; }
    ls = x32_sum(x16_sum(ls));
    if (lane < 4) *(LAS float*)(sm + L_RSUM + lane * 32 + w * 4) = ls;
    *(LAS v4u*)(sm + L_PF + w * 1024 + lane * 16) = v4u{pk2(s[0], s[1]), pk2(s[2], s[3]), pk2(s[4], s[5]), pk2(s[6], s[7])};
  }
  BAR();
  STAMP(3);
  tag = flg[4] & 0xff;
  v4u pf[8];
#pragma unroll
  for (int m = 0; m < 8; ++m) pf[m] = *(LAS v4u*)(sm + L_PF + m * 1024 + lane * 16);
  VMCNT(0);
  STAMP(4);
  BAR();
  {
    LAS char* p = sm + L_V + (4 * g + tq) * 256 + ((w ^ (4 * (g & 1) + tq)) << 5) + tp * 8;
    v4u vf[8];
#pragma unroll
    for (int m = 0; m < 8; ++m) vf[m] = trfrag(p + m * 8192, 4096);
    FENCE();
    v4f acc = {0, 0, 0, 0}, acc1 = {0, 0, 0, 0};
#pragma unroll
    for (int m = 0; m < 8; m += 2) { acc = mfma(pf[m], vf[m], acc); acc1 = mfma(pf[m + 1], vf[m + 1], acc1); }
    acc += acc1;
    v4u o = {enc(acc.x, tag), enc(acc.y, tag), enc(acc.z, tag), enc(acc.w, tag)};
    if (lane < 16)
      __builtin_amdgcn_raw_buffer_store_b128(o, rs, WS_OP + (((xc * 4 + (w >> 1)) * 8 + bq) * 4 + sp) * 512 + ((w & 1) * 16 + lane) * 16, 0, AUX_SC1);
  }
  STAMP(6);
  const int f8 = (tq & 3) | ((g & 1) << 2);
  if (isR) {
    BAR();
    v4u wof[4];
    {
      LAS char* p = sm + L_K + (8 * g + tq) * 256 + ((w ^ f8) << 5) + tp * 8;
#pragma unroll
      for (int m = 0; m < 4; ++m) wof[m] = trfrag(p + m * 8192, 1024);
    }
    {
      v4u a[2];
      const int base = WS_OP + ((xc * 4 + (r >> 2)) * 8 + w) * 2048 + lane * 16;
      const int sb = WS_ST + (xc * 8 + w) * 512 + (lane & 3) * 128;
      float fa[4], fb[4];
      bool have = false;
      for (unsigned sn = 0; sn < SPIN_MAX; ++sn) {
        FENCE();
        a[0] = __builtin_amdgcn_raw_buffer_load_b128(rs, base, 0, AUX_SC1);
        a[1] = __builtin_amdgcn_raw_buffer_load_b128(rs, base + 1024, 0, AUX_SC1);
        if (!have) {
          v4u mu = __builtin_amdgcn_raw_buffer_load_b128(rs, sb, 0, AUX_SC1);
          v4u lu = __builtin_amdgcn_raw_buffer_load_b128(rs, sb + 16, 0, AUX_SC1);
          if (__all(tag_ok(mu, tag) && tag_ok(lu, tag))) {
            have = true;
#pragma unroll
            for (int i = 0; i < 4; ++i) {
              float mi = dec(mu[i]), li = dec(lu[i]);
              float M = fmaxf(mi, dpp<0xB1>(mi)); M = fmaxf(M, dpp<0x4E>(M));
              float sc = __builtin_amdgcn_exp2f((mi - M) * 1.4426950408889634f);
              float L = li * sc; L += dpp<0xB1>(L); L += dpp<0x4E>(L);
              float fi = sc * frcp(L);
              float f0 = dpp<0x00>(fi), f1 = dpp<0x55>(fi), f2 = dpp<0xAA>(fi), f3 = dpp<0xFF>(fi);
              fa[i] = lane < 32 ? f0 : f1; fb[i] = lane < 32 ? f2 : f3;
            }
          }
        }
        if (have && __all(tag_ok(a[0], tag) && tag_ok(a[1], tag))) break;
      }
      STAMP(7);
      float ov[4];
#pragma unroll
      for (int i = 0; i < 4; ++i) ov[i] = x32_sum(dec(a[0][i]) * fa[i] + dec(a[1][i]) * fb[i]);
      if (lane < 32) *(LAS v2u*)(sm + L_OB + (lane >> 3) * 512 + ((lane & 7) >> 1) * 128 + w * 16 + (lane & 1) * 8) = v2u{pk2(ov[0], ov[1]), pk2(ov[2], ov[3])};
    }
    BAR();
    {
      v4u ob[4];
#pragma unroll
      for (int m = 0; m < 4; ++m) ob[m] = *(LAS v4u*)(sm + L_OB + m * 512 + g * 128 + (cl & 7) * 16);
      FENCE();
      v4f acc = {0, 0, 0, 0}, acc1 = {0, 0, 0, 0};
      acc = mfma(wof[0], ob[0], acc); acc1 = mfma(wof[1], ob[1], acc1);
      acc = mfma(wof[2], ob[2], acc); acc1 = mfma(wof[3], ob[3], acc1);
      acc += acc1;
      const int src = 2 * (lane >> 3) + ((lane >> 2) & 1) + 16 * (lane & 3);
      v4u o = {enc(bperm(src, acc.x), tag), enc(bperm(src, acc.y), tag), enc(bperm(src, acc.z), tag), enc(bperm(src, acc.w), tag)};
      if (lane < 32)
        __builtin_amdgcn_raw_buffer_store_b128(o, rs, WS_AP + (((lane >> 3) * 32 + 8 * (r & 3) + w) * 32 + (xc * 4 + (r >> 2))) * 128 + (lane & 7) * 16, 0, AUX_SC1);
    }
    STAMP(8);
  } else if (isM) {
    {
      const char* src = (const char*)w1 + (size_t)(64 * w + (lane >> 3)) * 3584 + hgM * 128;
      const int s0 = ((lane & 7) ^ (((lane >> 4) & 1) << 1)) << 4;
#pragma unroll
      for (int j = 0; j < 8; ++j) glds(src + (size_t)j * 8 * 3584 + (s0 ^ ((j & 1) << 6)), lds0 + L_K + (8 * w + j) * 1024);
    }
    BAR();
    {
      const char* src = (const char*)w2 + (size_t)(64 * hgM + 32 * (w >> 2) + 4 * (w & 3)) * 1024;
#pragma unroll
      for (int j = 0; j < 8; ++j) {
        int ff = (j & 3) | ((w & 1) << 2);
        glds(src + (size_t)(16 * (j >> 2) + (j & 3)) * 1024 + ((((lane >> 1) ^ ff) << 5) | ((lane & 1) << 4)), lds0 + L_V + (8 * w + j) * 1024);
      }
    }
    VMCNT(0);
    STAMP(9);
    BAR();
    v4u bf[8];
    LAS char* pw2 = sm + L_V + (8 * g + tq) * 1024 + tp * 8;
    if (w < 4) {
      v4u af[16];
      const int wf = ((tq >> 1) & 1) | ((g & 1) << 1);
      LAS char* p = sm + L_K + (8 * g + tq) * 128 + tp * 8 + ((w ^ wf) << 5);
#pragma unroll
      for (int st = 0; st < 16; ++st) af[st] = trfrag(p + st * 4096, 512);
      BAR();
      STAMP(11);
      v4u yb[16];
#pragma unroll
      for (int st = 0; st < 16; ++st) yb[st] = *(LAS v4u*)(sm + L_Y + (cl & 1) * 1024 + st * 64 + g * 16);
      FENCE();
      v4f acc = {0, 0, 0, 0}, acc1 = {0, 0, 0, 0};
#pragma unroll
      for (int st = 0; st < 16; st += 2) { acc = mfma(af[st], yb[st], acc); acc1 = mfma(af[st + 1], yb[st + 1], acc1); }
      FENCE();
#pragma unroll
      for (int j = 0; j < 4; ++j)
#pragma unroll
        for (int m = 0; m < 2; ++m) bf[2 * j + m] = trfrag(pw2 + (((4 * w + j) ^ f8) << 5) + m * 32768, 4096);
      acc += acc1;
      float h0 = gelu_t(rb(rb(acc.x) + lo_f(b1v.x))), h1 = gelu_t(rb(rb(acc.y) + hi_f(b1v.x)));
      float h2 = gelu_t(rb(rb(acc.z) + lo_f(b1v.y))), h3 = gelu_t(rb(rb(acc.w) + hi_f(b1v.y)));
      if (cl < 2) *(LAS v2u*)(sm + L_HB + (w >> 1) * 128 + g * 32 + cl * 16 + (w & 1) * 8) = v2u{pk2(h0, h1), pk2(h2, h3)};
    } else {
#pragma unroll
      for (int j = 0; j < 4; ++j)
#pragma unroll
        for (int m = 0; m < 2; ++m) bf[2 * j + m] = trfrag(pw2 + (((4 * w + j) ^ f8) << 5) + m * 32768, 4096);
      if (w < 6) {
        const int bl = w - 4;
        v4u a[2];
        const int base = WS_HH + bpM * 4096 + (lane >> 2) * 128 + bl * 64 + (lane & 3) * 16;
        for (unsigned sn = 0; sn < SPIN_MAX; ++sn) {
          FENCE();
          a[0] = __builtin_amdgcn_raw_buffer_load_b128(rs, base, 0, AUX_SC1);
          a[1] = __builtin_amdgcn_raw_buffer_load_b128(rs, base + 2048, 0, AUX_SC1);
          if (__all(tag_ok(a[0], tag) && tag_ok(a[1], tag))) break;
        }
        STAMP(10);
        float v[8] = {dec(a[0].x), dec(a[0].y), dec(a[0].z), dec(a[0].w), dec(a[1].x), dec(a[1].y), dec(a[1].z), dec(a[1].w)};
#pragma unroll
        for (int pass = 0; pass < 2; ++pass) {
          const float x0 = __builtin_bit_cast(float, __builtin_amdgcn_readfirstlane(__builtin_bit_cast(int, v[0])));
          float d[8];
#pragma unroll
          for (int i = 0; i < 8; ++i) d[i] = v[i] - x0;
          float s = ((d[0] + d[1]) + (d[2] + d[3])) + ((d[4] + d[5]) + (d[6] + d[7]));
          float q = ((d[0] * d[0] + d[1] * d[1]) + (d[2] * d[2] + d[3] * d[3])) + ((d[4] * d[4] + d[5] * d[5]) + (d[6] * d[6] + d[7] * d[7]));
          s += dpp<0xB1>(s); q += dpp<0xB1>(q); s += dpp<0x4E>(s); q += dpp<0x4E>(q); s += dpp<0x141>(s); q += dpp<0x141>(q);
          s += dpp<0x140>(s); q += dpp<0x140>(q); s = x16_sum(s); q = x16_sum(q); s = x32_sum(s); q = x32_sum(q);
          float md = s * (1.0f / 512.0f);
          float var = fmaxf(q * (1.0f / 512.0f) - md * md, 0.0f);
          float mb = rb(x0 + md), inv = frcp(rb(fsqrt(rb(rb(var) + 1e-5f))));
#pragma unroll
          for (int i = 0; i < 8; ++i) v[i] = rb(rb(v[i] - mb) * inv);
        }
        *(LAS v2u*)(sm + L_Y + bl * 1024 + lane * 8) = v2u{pk2(v[0], v[1]), pk2(v[2], v[3])};
        *(LAS v2u*)(sm + L_Y + bl * 1024 + 512 + lane * 8) = v2u{pk2(v[4], v[5]), pk2(v[6], v[7])};
      }
      BAR();
    }
    BAR();
    {
      v4u hb0 = *(LAS v4u*)(sm + L_HB + g * 32 + (cl & 1) * 16), hb1 = *(LAS v4u*)(sm + L_HB + 128 + g * 32 + (cl & 1) * 16);
      v4f acc[4];
#pragma unroll
      for (int j = 0; j < 4; ++j) { acc[j] = mfma(bf[2 * j], hb0, v4f{0, 0, 0, 0}); acc[j] = mfma(bf[2 * j + 1], hb1, acc[j]); }
      if (cl < 2) {
#pragma unroll
        for (int j = 0; j < 4; ++j) *(LAS v4f*)(sm + L_TB + w * 512 + j * 128 + cl * 64 + g * 16) = acc[j];
      }
      FENCE();
      v4f v = *(LAS v4f*)(sm + L_TB + w * 512 + (lane & 31) * 16);
      v4u o = {enc(v.x, tag), enc(v.y, tag), enc(v.z, tag), enc(v.w, tag)};
      if (lane < 32)
        __builtin_amdgcn_raw_buffer_store_b128(o, rs, WS_FP + ((bpM * 32 + 4 * w + (lane >> 3)) * 28 + hgM) * 128 + (lane & 7) * 16, 0, AUX_SC1);
    }
    STAMP(12);
  }
  FLUSH();
}

extern "C" void kernel_launch(void* const* d_in, const int* in_sizes, int n_in, void* d_out, int out_size, void* d_ws, size_t ws_size, hipStream_t stream) {
  fused_decode_kernel<<<256, 704, 0, stream>>>((const u16*)d_in[0], (const u16*)d_in[1], (const u16*)d_in[2], (const u16*)d_in[3], (const u16*)d_in[4],
                                               (const u16*)d_in[6], (const u16*)d_in[7], (const u16*)d_in[8], (const u16*)d_in[9], (u16*)d_out, (char*)d_ws);
}
```

```cpp
#define PADT 2800ull
#include <hip/hip_runtime.h>
#include <stdint.h>

typedef unsigned short u16;
typedef unsigned v4u __attribute__((ext_vector_type(4)));
typedef unsigned v2u __attribute__((ext_vector_type(2)));
typedef float v4f __attribute__((ext_vector_type(4)));
typedef short v4s __attribute__((ext_vector_type(4)));
typedef __bf16 v8b __attribute__((ext_vector_type(8)));
#define LAS __attribute__((address_space(3)))
#define DEV __device__ __forceinline__

constexpr int WS_EP = 0;
constexpr int WS_ST = 4096;
constexpr int WS_HH = WS_ST + 32768;
constexpr int WS_QP = WS_HH + 16384;
constexpr int WS_OP = WS_QP + 524288;
constexpr int WS_AP = WS_OP + 524288;
constexpr int WS_FP = WS_AP + 524288;
constexpr int WS_END = WS_FP + 128 * 28 * 128;
constexpr int L_K = 0, L_V = 65536, L_W = 131072, L_M = 147456;
constexpr int L_XLN = L_M, L_OB = L_M, L_QL = L_M + 2048, L_PF = L_M + 3072, L_TQ = L_PF, L_Y = L_PF, L_TB = L_PF + 2048, L_HB = L_M + 2048;
constexpr int L_RMAX = L_M + 11264, L_RSUM = L_M + 11392, L_FLG = L_M + 11552;
constexpr int LDS_BYTES = 163840;
constexpr int AUX_SC1 = 16;
constexpr unsigned SPIN_MAX = 200000u;

#define STAMP(i) do {} while (0)
#define FLUSH() do {} while (0)

DEV float bfu(unsigned hi16) { return __uint_as_float(hi16 << 16); }
DEV float lo_f(unsigned p) { return __uint_as_float(p << 16); }
DEV float hi_f(unsigned p) { return __uint_as_float(p & 0xffff0000u); }
typedef __bf16 v2b __attribute__((ext_vector_type(2)));
typedef float v2f __attribute__((ext_vector_type(2)));
DEV unsigned pk2(float lo, float hi) { return __builtin_bit_cast(unsigned, __builtin_convertvector(v2f{lo, hi}, v2b)); }
DEV float rb(float f) { return lo_f(pk2(f, f)); }
DEV unsigned enc(float v, unsigned t) { return ((__float_as_uint(v) + 0x80u) & 0xffffff00u) | t; }
DEV float dec(unsigned u) { return __uint_as_float(u & 0xffffff00u); }
DEV bool tag_ok(v4u x, unsigned t) { return ((((x.x ^ t) | (x.y ^ t)) | ((x.z ^ t) | (x.w ^ t))) & 0xffu) == 0; }
template <int C> DEV float dpp(float x) { return __builtin_bit_cast(float, __builtin_amdgcn_update_dpp(0, __builtin_bit_cast(int, x), C, 0xf, 0xf, true)); }
DEV float x16_sum(float x) { auto s = __builtin_amdgcn_permlane16_swap(__float_as_uint(x), __float_as_uint(x), false, false); return __uint_as_float(s[0]) + __uint_as_float(s[1]); }
DEV float x32_sum(float x) { auto s = __builtin_amdgcn_permlane32_swap(__float_as_uint(x), __float_as_uint(x), false, false); return __uint_as_float(s[0]) + __uint_as_float(s[1]); }
DEV float x16_max(float x) { auto s = __builtin_amdgcn_permlane16_swap(__float_as_uint(x), __float_as_uint(x), false, false); return fmaxf(__uint_as_float(s[0]), __uint_as_float(s[1])); }
DEV float x32_max(float x) { auto s = __builtin_amdgcn_permlane32_swap(__float_as_uint(x), __float_as_uint(x), false, false); return fmaxf(__uint_as_float(s[0]), __uint_as_float(s[1])); }
DEV float sum8(float x) { x += dpp<0xB1>(x); x += dpp<0x4E>(x); x += dpp<0x141>(x); return x; }
DEV float sum16(float x) { x = sum8(x); x += dpp<0x140>(x); return x; }
DEV float fsqrt(float x) { return __builtin_amdgcn_sqrtf(x); }
DEV float frcp(float x) { return __builtin_amdgcn_rcpf(x); }
#define THIN_D 1
#define STR2(x) #x
#define STR(x) STR2(x)
DEV float sum64(float x) { x = sum8(x); x += dpp<0x140>(x); x = x16_sum(x); return x32_sum(x); }
DEV void glds_nt(const void* g, unsigned lds) {
  unsigned keep;
  asm volatile("s_mov_b32 %0, m0\n\ts_mov_b32 m0, %2\n\ts_nop 0\n\tglobal_load_lds_dwordx4 %1, off nt\n\ts_mov_b32 m0, %0" : "=&s"(keep) : "v"(g), "s"(lds) : "memory");
}
DEV void glds(const void* g, unsigned lds) {
  unsigned keep;
  asm volatile("s_mov_b32 %0, m0\n\ts_mov_b32 m0, %2\n\ts_nop 0\n\tglobal_load_lds_dwordx4 %1, off\n\ts_mov_b32 m0, %0" : "=&s"(keep) : "v"(g), "s"(lds) : "memory");
}
#define BAR() asm volatile("s_waitcnt lgkmcnt(0)\n\ts_barrier" ::: "memory")
#define VMCNT(n) asm volatile("s_waitcnt vmcnt(" #n ")" ::: "memory")
#define FENCE() asm volatile("" ::: "memory")
DEV v8b mk(v4u a) { return __builtin_bit_cast(v8b, a); }
DEV v4f mfma(v4u a, v4u b, v4f c) { return __builtin_amdgcn_mfma_f32_16x16x32_bf16(mk(a), mk(b), c, 0, 0, 0); }
DEV v4u trfrag(LAS char* p, int off2) {
  v4s a = __builtin_amdgcn_ds_read_tr16_b64_v4i16((LAS v4s*)p);
  v4s b = __builtin_amdgcn_ds_read_tr16_b64_v4i16((LAS v4s*)(p + off2));
  v2u a2 = __builtin_bit_cast(v2u, a), b2 = __builtin_bit_cast(v2u, b);
  return v4u{a2.x, a2.y, b2.x, b2.y};
}
DEV void lds_wait(LAS unsigned* p, unsigned v) {
  for (unsigned s = 0; s < 50000000u; ++s) { if (*(volatile LAS unsigned*)p >= v) break; }
  FENCE();
}
DEV void lds_inc(LAS unsigned* p, int lane) {
  asm volatile("s_waitcnt lgkmcnt(0)" ::: "memory");
  if (lane == 0) __hip_atomic_fetch_add(p, 1u, __ATOMIC_RELAXED, __HIP_MEMORY_SCOPE_WORKGROUP);
  FENCE();
}
DEV float bperm(int srclane, float v) { return __builtin_bit_cast(float, __builtin_amdgcn_ds_bpermute(srclane << 2, __builtin_bit_cast(int, v))); }
DEV float gelu_t(float z) {
  float u = 0.7978845608028654f * (z + 0.044715f * z * z * z);
  float e = __builtin_amdgcn_exp2f(-2.885390081777927f * u);
  return z * __builtin_amdgcn_rcpf(1.0f + e);
}

__global__ __launch_bounds__(704) void fused_decode_kernel(const u16* __restrict__ x_, const u16* __restrict__ kc_, const u16* __restrict__ vc_,
    const u16* __restrict__ wq_, const u16* __restrict__ wo_, const u16* __restrict__ w1_, const u16* __restrict__ b1_, const u16* __restrict__ w2_,
    const u16* __restrict__ b2_, u16* __restrict__ out_, char* ws_) {
  typedef __attribute__((address_space(4))) unsigned long long KA;
  KA* ka = (KA*)__builtin_amdgcn_kernarg_segment_ptr();
  unsigned long long tstart;
  asm volatile("s_memrealtime %0\n\ts_waitcnt lgkmcnt(0)" : "=s"(tstart), "+s"(ka) :: "memory");
  typedef __attribute__((address_space(1))) u16 GU; typedef __attribute__((address_space(1))) char GC;
  const u16* x = (const u16*)(GU*)ka[0]; const u16* kc = (const u16*)(GU*)ka[1]; const u16* vc = (const u16*)(GU*)ka[2];
  const u16* wq = (const u16*)(GU*)ka[3]; const u16* wo = (const u16*)(GU*)ka[4]; const u16* w1 = (const u16*)(GU*)ka[5];
  const u16* b1 = (const u16*)(GU*)ka[6]; const u16* w2 = (const u16*)(GU*)ka[7]; const u16* b2 = (const u16*)(GU*)ka[8];
  u16* out = (u16*)(GU*)ka[9]; char* ws = (char*)(GC*)ka[10];
  __shared__ __attribute__((aligned(1024))) char smem_[LDS_BYTES];
  LAS char* sm = (LAS char*)smem_;
  const unsigned lds0 = (unsigned)(size_t)sm;
  const int lane = threadIdx.x & 63, w = __builtin_amdgcn_readfirstlane(threadIdx.x >> 6);
  const int c = blockIdx.x, xc = c & 7, r = c >> 3;
  const int bq = r >> 2, sp = r & 3;
  const int cl = lane & 15, g = lane >> 4;
  __amdgpu_buffer_rsrc_t rs = __builtin_amdgcn_make_buffer_rsrc(ws, 0, WS_END, 0x00020000);
  LAS unsigned* flg = (LAS unsigned*)(sm + L_FLG);
  STAMP(0);
  const bool isR = r < 16, isM = (r >= 16) && (r < 30);
  const int bpR = r >> 2, cgR = 4 * xc + (r & 3);
  const int im = 14 * xc + (r - 16), hgM = im >> 2, bpM = im & 3;
  const int tq = (lane >> 2) & 3, tp = lane & 3;
  unsigned tag = 0;

  if (w == 8) {
    __builtin_amdgcn_s_setprio(3);
    unsigned E = __builtin_amdgcn_raw_buffer_load_b32(rs, WS_EP, 0, AUX_SC1);
    {
      const int kq = r & 3, cg = r >> 2;
      const char* src = (const char*)wq + (size_t)(128 * kq + (lane >> 3)) * 8192 + (512 * xc + 64 * cg) * 2;
      const int s0 = ((lane & 7) ^ (((lane >> 4) & 1) << 1)) << 4;
#pragma unroll
      for (int j = 0; j < 16; ++j) glds_nt(src + (size_t)j * 8 * 8192 + (s0 ^ ((j & 1) << 6)), lds0 + L_W + j * 1024);
    }
    if (lane < 8) flg[lane] = 0;
    BAR();
    VMCNT(0);
    STAMP(1);
    E = __builtin_amdgcn_readfirstlane(E);
    tag = (E + 1) & 0xff;
    if (lane == 0) flg[4] = tag | 0x100;
    {
      v4u wa[16];
      const int wf = ((tq >> 1) & 1) | ((g & 1) << 1);
#pragma unroll
      for (int t = 0; t < 4; ++t) {
        LAS char* p = sm + L_W + (8 * g + tq) * 128 + tp * 8 + ((t ^ wf) << 5);
#pragma unroll
        for (int st = 0; st < 4; ++st) wa[4 * t + st] = trfrag(p + st * 4096, 512);
      }
      lds_wait(flg + 0, 2);
      v4u xb[4];
#pragma unroll
      for (int st = 0; st < 4; ++st) xb[st] = *(LAS v4u*)(sm + L_XLN + (cl & 7) * 256 + st * 64 + g * 16);
      FENCE();
      v4f acc[4] = {v4f{0, 0, 0, 0}, v4f{0, 0, 0, 0}, v4f{0, 0, 0, 0}, v4f{0, 0, 0, 0}};
#pragma unroll
      for (int st = 0; st < 4; ++st)
#pragma unroll
        for (int t = 0; t < 4; ++t) acc[t] = mfma(wa[4 * t + st], xb[st], acc[t]);
      if (cl < 8) {
#pragma unroll
        for (int t = 0; t < 4; ++t) *(LAS v4f*)(sm + L_TQ + cl * 272 + (16 * t + 4 * g) * 4) = acc[t];
      }
      FENCE();
      const int kq = r & 3, cg = r >> 2;
#pragma unroll
      for (int i = 0; i < 2; ++i) {
        int idx = 64 * i + lane, b = idx >> 4, u = idx & 15;
        v4f v = *(LAS v4f*)(sm + L_TQ + b * 272 + u * 16);
        v4u o = {enc(v.x, tag), enc(v.y, tag), enc(v.z, tag), enc(v.w, tag)};
        __builtin_amdgcn_raw_buffer_store_b128(o, rs, WS_QP + ((xc * 8 + b) * 4 + kq) * 2048 + cg * 256 + u * 16, 0, AUX_SC1);
      }
    }
    STAMP(2);
    {
      v4u q[8];
      const int base = WS_QP + (xc * 8 + bq) * 8192 + lane * 16;
      for (unsigned sn = 0; sn < SPIN_MAX; ++sn) {
        FENCE();
        bool ok = true;
#pragma unroll
        for (int i = 0; i < 8; ++i) q[i] = __builtin_amdgcn_raw_buffer_load_b128(rs, base + i * 1024, 0, AUX_SC1);
#pragma unroll
        for (int i = 0; i < 8; ++i) ok &= tag_ok(q[i], tag);
        if (__all(ok)) break;
      }
#pragma unroll
      for (int hf = 0; hf < 2; ++hf) {
        float a0 = (dec(q[hf].x) + dec(q[2 + hf].x)) + (dec(q[4 + hf].x) + dec(q[6 + hf].x));
        float a1 = (dec(q[hf].y) + dec(q[2 + hf].y)) + (dec(q[4 + hf].y) + dec(q[6 + hf].y));
        float a2 = (dec(q[hf].z) + dec(q[2 + hf].z)) + (dec(q[4 + hf].z) + dec(q[6 + hf].z));
        float a3 = (dec(q[hf].w) + dec(q[2 + hf].w)) + (dec(q[4 + hf].w) + dec(q[6 + hf].w));
        *(LAS v2u*)(sm + L_QL + (hf * 256 + 4 * lane) * 2) = v2u{pk2(a0, a1), pk2(a2, a3)};
      }
      lds_inc(flg + 1, lane);
    }
    STAMP(3);
    v2u xres = {0, 0};
    unsigned b2lo = 0, b2hi = 0;
    if (isR) {
      xres = *(const v2u*)(x + (2 * bpR + ((lane >> 2) & 1)) * 512 + 16 * cgR + 4 * (lane & 3));
      v2u bb = *(const v2u*)(b2 + 16 * cgR + 4 * (lane & 3));
      b2lo = bb.x; b2hi = bb.y;
    }
    BAR();
    BAR();
    {
      const int h = lane & 3;
      v4f m0 = *(LAS v4f*)(sm + L_RMAX + h * 32), m1 = *(LAS v4f*)(sm + L_RMAX + h * 32 + 16);
      v4f s0 = *(LAS v4f*)(sm + L_RSUM + h * 32), s1 = *(LAS v4f*)(sm + L_RSUM + h * 32 + 16);
      float m = fmaxf(fmaxf(fmaxf(m0.x, m0.y), fmaxf(m0.z, m0.w)), fmaxf(fmaxf(m1.x, m1.y), fmaxf(m1.z, m1.w)));
      float l = ((s0.x + s0.y) + (s0.z + s0.w)) + ((s1.x + s1.y) + (s1.z + s1.w));
      float v = (lane & 4) ? l : m;
      v4u o = {enc(dpp<0x00>(v), tag), enc(dpp<0x55>(v), tag), enc(dpp<0xAA>(v), tag), enc(dpp<0xFF>(v), tag)};
      if (lane == 0 || lane == 4) __builtin_amdgcn_raw_buffer_store_b128(o, rs, WS_ST + (xc * 8 + bq) * 512 + sp * 128 + (lane >> 2) * 16, 0, AUX_SC1);
    }
    STAMP(4);
    BAR();
    if (isM) { BAR(); BAR(); BAR(); BAR(); FLUSH(); return; }
    if (!isR) { FLUSH(); return; }
    BAR();
    BAR();
    float h0, h1, h2, h3;
    {
      v4u a[4];
      const int base = WS_AP + (bpR * 32 + cgR) * 4096 + lane * 16;
      for (unsigned sn = 0; sn < SPIN_MAX; ++sn) {
        FENCE();
        bool ok = true;
#pragma unroll
        for (int i = 0; i < 4; ++i) a[i] = __builtin_amdgcn_raw_buffer_load_b128(rs, base + i * 1024, 0, AUX_SC1);
#pragma unroll
        for (int i = 0; i < 4; ++i) ok &= tag_ok(a[i], tag);
        if (__all(ok)) break;
      }
      STAMP(9);
      float s[4] = {(dec(a[0].x) + dec(a[1].x)) + (dec(a[2].x) + dec(a[3].x)), (dec(a[0].y) + dec(a[1].y)) + (dec(a[2].y) + dec(a[3].y)),
                    (dec(a[0].z) + dec(a[1].z)) + (dec(a[2].z) + dec(a[3].z)), (dec(a[0].w) + dec(a[1].w)) + (dec(a[2].w) + dec(a[3].w))};
#pragma unroll
      for (int i = 0; i < 4; ++i) { s[i] += dpp<0x128>(s[i]); s[i] = x16_sum(s[i]); s[i] = x32_sum(s[i]); }
      h0 = rb(lo_f(xres.x) + rb(s[0])); h1 = rb(hi_f(xres.x) + rb(s[1])); h2 = rb(lo_f(xres.y) + rb(s[2])); h3 = rb(hi_f(xres.y) + rb(s[3]));
      v4u o = {__float_as_uint(h0) | tag, __float_as_uint(h1) | tag, __float_as_uint(h2) | tag, __float_as_uint(h3) | tag};
      if (lane < 8) __builtin_amdgcn_raw_buffer_store_b128(o, rs, WS_HH + (bpR * 32 + cgR) * 128 + lane * 16, 0, AUX_SC1);
    }
    STAMP(10);
    {
      v4u a[4];
      const int base = WS_FP + (bpR * 32 + cgR) * (28 * 128);
      const int o3 = (lane < 32 ? 192 + lane : 128 + lane) * 16;
      for (unsigned sn = 0; sn < SPIN_MAX; ++sn) {
        FENCE();
        bool ok = true;
#pragma unroll
        for (int i = 0; i < 3; ++i) a[i] = __builtin_amdgcn_raw_buffer_load_b128(rs, base + i * 1024 + lane * 16, 0, AUX_SC1);
        a[3] = __builtin_amdgcn_raw_buffer_load_b128(rs, base + o3, 0, AUX_SC1);
#pragma unroll
        for (int i = 0; i < 4; ++i) ok &= tag_ok(a[i], tag);
        if (__all(ok)) break;
      }
      STAMP(11);
      const float k3 = lane < 32 ? 1.0f : 0.0f;
      float s[4] = {(dec(a[0].x) + dec(a[1].x)) + (dec(a[2].x) + k3 * dec(a[3].x)), (dec(a[0].y) + dec(a[1].y)) + (dec(a[2].y) + k3 * dec(a[3].y)),
                    (dec(a[0].z) + dec(a[1].z)) + (dec(a[2].z) + k3 * dec(a[3].z)), (dec(a[0].w) + dec(a[1].w)) + (dec(a[2].w) + k3 * dec(a[3].w))};
#pragma unroll
      for (int i = 0; i < 4; ++i) { s[i] += dpp<0x128>(s[i]); s[i] = x16_sum(s[i]); s[i] = x32_sum(s[i]); }
      float o0 = h0 + rb(rb(s[0]) + lo_f(b2lo)), o1 = h1 + rb(rb(s[1]) + hi_f(b2lo)), o2 = h2 + rb(rb(s[2]) + lo_f(b2hi)), o3f = h3 + rb(rb(s[3]) + hi_f(b2hi));
      if (lane < 8) *(v2u*)(out + (2 * bpR + (lane >> 2)) * 512 + 16 * cgR + 4 * (lane & 3)) = v2u{pk2(o0, o1), pk2(o2, o3f)};
      if (c == 0 && lane == 0) __builtin_amdgcn_raw_buffer_store_b32(E + 1, rs, WS_EP, 0, AUX_SC1);
    }
    if (c == 0) for (unsigned sn = 0; sn < 100000u; ++sn) { if (__builtin_amdgcn_s_memrealtime() - tstart >= PADT) break; }
    STAMP(12);
    FLUSH();
    return;
  }
  if (w >= 9) {
    __builtin_amdgcn_s_setprio(2);
    v4u xr[4], xs;
    const int xb_ = 4 * (w - 9) + g;
    const u16* xp = x + xb_ * 512 + cl * 8;
#pragma unroll
    for (int i = 0; i < 4; ++i) xr[i] = *(const v4u*)(xp + 128 * i);
    xs = *(const v4u*)(xp + 128 * (r & 3));
    BAR();
    {
      v2f f[16], s2[4] = {v2f{0, 0}, v2f{0, 0}, v2f{0, 0}, v2f{0, 0}};
#pragma unroll
      for (int i = 0; i < 16; ++i) { f[i] = v2f{lo_f(xr[i >> 2][i & 3]), hi_f(xr[i >> 2][i & 3])}; s2[i & 3] += f[i]; }
      v2f st = (s2[0] + s2[1]) + (s2[2] + s2[3]);
      float mean = sum16(st.x + st.y) * (1.0f / 512.0f);
      v2f q2[4] = {v2f{0, 0}, v2f{0, 0}, v2f{0, 0}, v2f{0, 0}}, mm = {mean, mean};
#pragma unroll
      for (int i = 0; i < 16; ++i) { v2f d = f[i] - mm; q2[i & 3] = __builtin_elementwise_fma(d, d, q2[i & 3]); }
      v2f qt = (q2[0] + q2[1]) + (q2[2] + q2[3]);
      float var = sum16(qt.x + qt.y) * (1.0f / 512.0f);
      float mb = rb(mean), inv = frcp(rb(fsqrt(rb(rb(var) + 1e-5f))));
      v4u o;
#pragma unroll
      for (int j = 0; j < 4; ++j) o[j] = pk2(rb(lo_f(xs[j]) - mb) * inv, rb(hi_f(xs[j]) - mb) * inv);
      *(LAS v4u*)(sm + L_XLN + xb_ * 256 + cl * 16) = o;
      lds_inc(flg + 0, lane);
    }
    if (w == 10) { FLUSH(); return; }
    STAMP(1);
    __builtin_amdgcn_s_setprio(0);
    BAR();
    BAR();
    if (isR) {
      const int qp = r >> 2, cg = r & 3;
      const char* src = (const char*)wo + (size_t)(512 * xc + 128 * g + 32 * qp) * 1024 + cg * 256;
      const int pos = lane & 15;
#pragma unroll 4
      for (int j = 0; j < 32; ++j) {
        int f8 = g | (((j >> 1) & 1) << 2);
        glds_nt(src + (size_t)(8 * (j >> 3) + 2 * ((j >> 1) & 3) + (j & 1)) * 1024 + ((((pos >> 1) ^ f8) << 5) | ((pos & 1) << 4)), lds0 + L_K + j * 1024);
      }
      STAMP(2);
      BAR();
      VMCNT(0);
      STAMP(3);
      BAR();
    } else {
      BAR();
    }
    FLUSH();
    return;
  }
  v2u b1v = {0, 0};
  if (isM && w < 4) b1v = *(const v2u*)(b1 + 64 * hgM + 16 * w + 4 * g);
  BAR();
  __builtin_amdgcn_s_setprio(1);
  {
    bool full = false;
    const char* kb = (const char*)kc + ((size_t)(bq * 8 + xc) * 4096 + 3072 + sp * 256 + 32 * w) * 256;
#pragma unroll
    for (int i = 0; i < 8; ++i) {
      int key = 4 * i + g;
      glds_nt(kb + key * 256 + (((lane & 15) ^ (key & 15)) << 4), lds0 + L_K + (32 * w + 4 * i) * 256);
      if (THIN_D > 0 && i >= THIN_D && !full) { asm volatile("s_waitcnt vmcnt(" STR(THIN_D) ")" ::: "memory"); full = __builtin_amdgcn_readfirstlane(*(volatile LAS unsigned*)(flg + 1)) != 0; }
    }
    __builtin_amdgcn_s_setprio(0);
    const char* vb = (const char*)vc + ((size_t)(bq * 8 + xc) * 4096 + 3072 + sp * 256 + 32 * w) * 256;
    const int pos = lane & 15;
    const int o0 = g * 256 + ((((pos >> 1) ^ g) << 5) | ((pos & 1) << 4));
    const int o1 = g * 256 + ((((pos >> 1) ^ (4 + g)) << 5) | ((pos & 1) << 4));
#pragma unroll
    for (int j = 0; j < 8; ++j) {
      glds_nt(vb + j * 1024 + ((j & 1) ? o1 : o0), lds0 + L_V + (8 * w + j) * 1024);
      if (THIN_D > 0 && j < 7 && !full) { asm volatile("s_waitcnt vmcnt(" STR(THIN_D) ")" ::: "memory"); full = __builtin_amdgcn_readfirstlane(*(volatile LAS unsigned*)(flg + 1)) != 0; }
    }
  }
  VMCNT(8);
  STAMP(1);
  lds_wait(flg + 1, 1);
  STAMP(2);
  __builtin_amdgcn_s_setprio(2);
  {
    v4f acc[2] = {v4f{0, 0, 0, 0}, v4f{0, 0, 0, 0}};
    v4u qf[4], kf[8];
#pragma unroll
    for (int st = 0; st < 4; ++st) {
      qf[st] = *(LAS v4u*)(sm + L_QL + (cl & 3) * 256 + st * 64 + g * 16);
#pragma unroll
      for (int tt = 0; tt < 2; ++tt) kf[2 * st + tt] = *(LAS v4u*)(sm + L_K + (32 * w + 16 * tt + cl) * 256 + (((4 * st + g) ^ cl) << 4));
    }
    FENCE();
#pragma unroll
    for (int st = 0; st < 4; ++st)
#pragma unroll
      for (int tt = 0; tt < 2; ++tt) acc[tt] = mfma(kf[2 * st + tt], qf[st], acc[tt]);
    float s[8];
#pragma unroll
    for (int i = 0; i < 8; ++i) s[i] = rb(rb(acc[i >> 2][i & 3]) * 0.08838834764831845f);
    float mx = fmaxf(fmaxf(fmaxf(s[0], s[1]), fmaxf(s[2], s[3])), fmaxf(fmaxf(s[4], s[5]), fmaxf(s[6], s[7])));
    mx = x32_max(x16_max(mx));
    if (lane < 4) *(LAS float*)(sm + L_RMAX + lane * 32 + w * 4) = mx;
    BAR();
    v4f m0 = *(LAS v4f*)(sm + L_RMAX + (cl & 3) * 32), m1 = *(LAS v4f*)(sm + L_RMAX + (cl & 3) * 32 + 16);
    float m = fmaxf(fmaxf(fmaxf(m0.x, m0.y), fmaxf(m0.z, m0.w)), fmaxf(fmaxf(m1.x, m1.y), fmaxf(m1.z, m1.w)));
    float ls = 0;
#pragma unroll
    for (int i = 0; i < 8; ++i) { s[i] = __builtin_amdgcn_exp2f((s[i] - m) * 1.4426950408889634f); ls += s[i]; }
    ls = x32_sum(x16_sum(ls));
    if (lane < 4) *(LAS float*)(sm + L_RSUM + lane * 32 + w * 4) = ls;
    *(LAS v4u*)(sm + L_PF + w * 1024 + lane * 16) = v4u{pk2(s[0], s[1]), pk2(s[2], s[3]), pk2(s[4], s[5]), pk2(s[6], s[7])};
  }
  BAR();
  STAMP(3);
  tag = flg[4] & 0xff;
  v4u pf[8];
#pragma unroll
  for (int m = 0; m < 8; ++m) pf[m] = *(LAS v4u*)(sm + L_PF + m * 1024 + lane * 16);
  VMCNT(0);
  STAMP(4);
  BAR();
  {
    LAS char* p = sm + L_V + (4 * g + tq) * 256 + ((w ^ (4 * (g & 1) + tq)) << 5) + tp * 8;
    v4u vf[8];
#pragma unroll
    for (int m = 0; m < 8; ++m) vf[m] = trfrag(p + m * 8192, 4096);
    FENCE();
    v4f acc = {0, 0, 0, 0}, acc1 = {0, 0, 0, 0};
#pragma unroll
    for (int m = 0; m < 8; m += 2) { acc = mfma(pf[m], vf[m], acc); acc1 = mfma(pf[m + 1], vf[m + 1], acc1); }
    acc += acc1;
    v4u o = {enc(acc.x, tag), enc(acc.y, tag), enc(acc.z, tag), enc(acc.w, tag)};
    if (lane < 16)
      __builtin_amdgcn_raw_buffer_store_b128(o, rs, WS_OP + (((xc * 4 + (w >> 1)) * 8 + bq) * 4 + sp) * 512 + ((w & 1) * 16 + lane) * 16, 0, AUX_SC1);
  }
  STAMP(6);
  const int f8 = (tq & 3) | ((g & 1) << 2);
  if (isR) {
    BAR();
    v4u wof[4];
    {
      LAS char* p = sm + L_K + (8 * g + tq) * 256 + ((w ^ f8) << 5) + tp * 8;
#pragma unroll
      for (int m = 0; m < 4; ++m) wof[m] = trfrag(p + m * 8192, 1024);
    }
    {
      v4u a[2];
      const int base = WS_OP + ((xc * 4 + (r >> 2)) * 8 + w) * 2048 + lane * 16;
      const int sb = WS_ST + (xc * 8 + w) * 512 + (lane & 3) * 128;
      float fa[4], fb[4];
      bool have = false;
      for (unsigned sn = 0; sn < SPIN_MAX; ++sn) {
        FENCE();
        a[0] = __builtin_amdgcn_raw_buffer_load_b128(rs, base, 0, AUX_SC1);
        a[1] = __builtin_amdgcn_raw_buffer_load_b128(rs, base + 1024, 0, AUX_SC1);
        if (!have) {
          v4u mu = __builtin_amdgcn_raw_buffer_load_b128(rs, sb, 0, AUX_SC1);
          v4u lu = __builtin_amdgcn_raw_buffer_load_b128(rs, sb + 16, 0, AUX_SC1);
          if (__all(tag_ok(mu, tag) && tag_ok(lu, tag))) {
            have = true;
#pragma unroll
            for (int i = 0; i < 4; ++i) {
              float mi = dec(mu[i]), li = dec(lu[i]);
              float M = fmaxf(mi, dpp<0xB1>(mi)); M = fmaxf(M, dpp<0x4E>(M));
              float sc = __builtin_amdgcn_exp2f((mi - M) * 1.4426950408889634f);
              float L = li * sc; L += dpp<0xB1>(L); L += dpp<0x4E>(L);
              float fi = sc * frcp(L);
              float f0 = dpp<0x00>(fi), f1 = dpp<0x55>(fi), f2 = dpp<0xAA>(fi), f3 = dpp<0xFF>(fi);
              fa[i] = lane < 32 ? f0 : f1; fb[i] = lane < 32 ? f2 : f3;
            }
          }
        }
        if (have && __all(tag_ok(a[0], tag) && tag_ok(a[1], tag))) break;
      }
      STAMP(7);
      float ov[4];
#pragma unroll
      for (int i = 0; i < 4; ++i) ov[i] = x32_sum(dec(a[0][i]) * fa[i] + dec(a[1][i]) * fb[i]);
      if (lane < 32) *(LAS v2u*)(sm + L_OB + (lane >> 3) * 512 + ((lane & 7) >> 1) * 128 + w * 16 + (lane & 1) * 8) = v2u{pk2(ov[0], ov[1]), pk2(ov[2], ov[3])};
    }
    BAR();
    {
      v4u ob[4];
#pragma unroll
      for (int m = 0; m < 4; ++m) ob[m] = *(LAS v4u*)(sm + L_OB + m * 512 + g * 128 + (cl & 7) * 16);
      FENCE();
      v4f acc = {0, 0, 0, 0}, acc1 = {0, 0, 0, 0};
      acc = mfma(wof[0], ob[0], acc); acc1 = mfma(wof[1], ob[1], acc1);
      acc = mfma(wof[2], ob[2], acc); acc1 = mfma(wof[3], ob[3], acc1);
      acc += acc1;
      const int src = 2 * (lane >> 3) + ((lane >> 2) & 1) + 16 * (lane & 3);
      v4u o = {enc(bperm(src, acc.x), tag), enc(bperm(src, acc.y), tag), enc(bperm(src, acc.z), tag), enc(bperm(src, acc.w), tag)};
      if (lane < 32)
        __builtin_amdgcn_raw_buffer_store_b128(o, rs, WS_AP + (((lane >> 3) * 32 + 8 * (r & 3) + w) * 32 + (xc * 4 + (r >> 2))) * 128 + (lane & 7) * 16, 0, AUX_SC1);
    }
    STAMP(8);
  } else if (isM) {
    {
      const char* src = (const char*)w1 + (size_t)(64 * w + (lane >> 3)) * 3584 + hgM * 128;
      const int s0 = ((lane & 7) ^ (((lane >> 4) & 1) << 1)) << 4;
#pragma unroll
      for (int j = 0; j < 8; ++j) glds(src + (size_t)j * 8 * 3584 + (s0 ^ ((j & 1) << 6)), lds0 + L_K + (8 * w + j) * 1024);
    }
    BAR();
    {
      const char* src = (const char*)w2 + (size_t)(64 * hgM + 32 * (w >> 2) + 4 * (w & 3)) * 1024;
#pragma unroll
      for (int j = 0; j < 8; ++j) {
        int ff = (j & 3) | ((w & 1) << 2);
        glds(src + (size_t)(16 * (j >> 2) + (j & 3)) * 1024 + ((((lane >> 1) ^ ff) << 5) | ((lane & 1) << 4)), lds0 + L_V + (8 * w + j) * 1024);
      }
    }
    VMCNT(0);
    STAMP(9);
    BAR();
    v4u bf[8];
    LAS char* pw2 = sm + L_V + (8 * g + tq) * 1024 + tp * 8;
    if (w < 4) {
      v4u af[16];
      const int wf = ((tq >> 1) & 1) | ((g & 1) << 1);
      LAS char* p = sm + L_K + (8 * g + tq) * 128 + tp * 8 + ((w ^ wf) << 5);
#pragma unroll
      for (int st = 0; st < 16; ++st) af[st] = trfrag(p + st * 4096, 512);
      BAR();
      STAMP(11);
      v4u yb[16];
#pragma unroll
      for (int st = 0; st < 16; ++st) yb[st] = *(LAS v4u*)(sm + L_Y + (cl & 1) * 1024 + st * 64 + g * 16);
      FENCE();
      v4f acc = {0, 0, 0, 0}, acc1 = {0, 0, 0, 0};
#pragma unroll
      for (int st = 0; st < 16; st += 2) { acc = mfma(af[st], yb[st], acc); acc1 = mfma(af[st + 1], yb[st + 1], acc1); }
      FENCE();
#pragma unroll
      for (int j = 0; j < 4; ++j)
#pragma unroll
        for (int m = 0; m < 2; ++m) bf[2 * j + m] = trfrag(pw2 + (((4 * w + j) ^ f8) << 5) + m * 32768, 4096);
      acc += acc1;
      float h0 = gelu_t(rb(rb(acc.x) + lo_f(b1v.x))), h1 = gelu_t(rb(rb(acc.y) + hi_f(b1v.x)));
      float h2 = gelu_t(rb(rb(acc.z) + lo_f(b1v.y))), h3 = gelu_t(rb(rb(acc.w) + hi_f(b1v.y)));
      if (cl < 2) *(LAS v2u*)(sm + L_HB + (w >> 1) * 128 + g * 32 + cl * 16 + (w & 1) * 8) = v2u{pk2(h0, h1), pk2(h2, h3)};
    } else {
#pragma unroll
      for (int j = 0; j < 4; ++j)
#pragma unroll
        for (int m = 0; m < 2; ++m) bf[2 * j + m] = trfrag(pw2 + (((4 * w + j) ^ f8) << 5) + m * 32768, 4096);
      if (w < 6) {
        const int bl = w - 4;
        v4u a[2];
        const int base = WS_HH + bpM * 4096 + (lane >> 2) * 128 + bl * 64 + (lane & 3) * 16;
        for (unsigned sn = 0; sn < SPIN_MAX; ++sn) {
          FENCE();
          a[0] = __builtin_amdgcn_raw_buffer_load_b128(rs, base, 0, AUX_SC1);
          a[1] = __builtin_amdgcn_raw_buffer_load_b128(rs, base + 2048, 0, AUX_SC1);
          if (__all(tag_ok(a[0], tag) && tag_ok(a[1], tag))) break;
        }
        STAMP(10);
        float v[8] = {dec(a[0].x), dec(a[0].y), dec(a[0].z), dec(a[0].w), dec(a[1].x), dec(a[1].y), dec(a[1].z), dec(a[1].w)};
#pragma unroll
        for (int pass = 0; pass < 2; ++pass) {
          const float x0 = __builtin_bit_cast(float, __builtin_amdgcn_readfirstlane(__builtin_bit_cast(int, v[0])));
          float d[8];
#pragma unroll
          for (int i = 0; i < 8; ++i) d[i] = v[i] - x0;
          float s = ((d[0] + d[1]) + (d[2] + d[3])) + ((d[4] + d[5]) + (d[6] + d[7]));
          float q = ((d[0] * d[0] + d[1] * d[1]) + (d[2] * d[2] + d[3] * d[3])) + ((d[4] * d[4] + d[5] * d[5]) + (d[6] * d[6] + d[7] * d[7]));
          s += dpp<0xB1>(s); q += dpp<0xB1>(q); s += dpp<0x4E>(s); q += dpp<0x4E>(q); s += dpp<0x141>(s); q += dpp<0x141>(q);
          s += dpp<0x140>(s); q += dpp<0x140>(q); s = x16_sum(s); q = x16_sum(q); s = x32_sum(s); q = x32_sum(q);
          float md = s * (1.0f / 512.0f);
          float var = fmaxf(q * (1.0f / 512.0f) - md * md, 0.0f);
          float mb = rb(x0 + md), inv = frcp(rb(fsqrt(rb(rb(var) + 1e-5f))));
#pragma unroll
          for (int i = 0; i < 8; ++i) v[i] = rb(rb(v[i] - mb) * inv);
        }
        *(LAS v2u*)(sm + L_Y + bl * 1024 + lane * 8) = v2u{pk2(v[0], v[1]), pk2(v[2], v[3])};
        *(LAS v2u*)(sm + L_Y + bl * 1024 + 512 + lane * 8) = v2u{pk2(v[4], v[5]), pk2(v[6], v[7])};
      }
      BAR();
    }
    BAR();
    {
      v4u hb0 = *(LAS v4u*)(sm + L_HB + g * 32 + (cl & 1) * 16), hb1 = *(LAS v4u*)(sm + L_HB + 128 + g * 32 + (cl & 1) * 16);
      v4f acc[4];
#pragma unroll
      for (int j = 0; j < 4; ++j) { acc[j] = mfma(bf[2 * j], hb0, v4f{0, 0, 0, 0}); acc[j] = mfma(bf[2 * j + 1], hb1, acc[j]); }
      if (cl < 2) {
#pragma unroll
        for (int j = 0; j < 4; ++j) *(LAS v4f*)(sm + L_TB + w * 512 + j * 128 + cl * 64 + g * 16) = acc[j];
      }
      FENCE();
      v4f v = *(LAS v4f*)(sm + L_TB + w * 512 + (lane & 31) * 16);
      v4u o = {enc(v.x, tag), enc(v.y, tag), enc(v.z, tag), enc(v.w, tag)};
      if (lane < 32)
        __builtin_amdgcn_raw_buffer_store_b128(o, rs, WS_FP + ((bpM * 32 + 4 * w + (lane >> 3)) * 28 + hgM) * 128 + (lane & 7) * 16, 0, AUX_SC1);
    }
    STAMP(12);
  }
  FLUSH();
}

extern "C" void kernel_launch(void* const* d_in, const int* in_sizes, int n_in, void* d_out, int out_size, void* d_ws, size_t ws_size, hipStream_t stream) {
  fused_decode_kernel<<<256, 704, 0, stream>>>((const u16*)d_in[0], (const u16*)d_in[1], (const u16*)d_in[2], (const u16*)d_in[3], (const u16*)d_in[4],
                                               (const u16*)d_in[6], (const u16*)d_in[7], (const u16*)d_in[8], (const u16*)d_in[9], (u16*)d_out, (char*)d_ws);
}
```

```cpp
#include <hip/hip_runtime.h>
#include <stdint.h>

typedef unsigned short u16;
typedef unsigned v4u __attribute__((ext_vector_type(4)));
typedef unsigned v2u __attribute__((ext_vector_type(2)));
typedef float v4f __attribute__((ext_vector_type(4)));
typedef short v4s __attribute__((ext_vector_type(4)));
typedef __bf16 v8b __attribute__((ext_vector_type(8)));
#define LAS __attribute__((address_space(3)))
#define DEV __device__ __forceinline__

constexpr int WS_EP = 0;
constexpr int WS_ST = 4096;
constexpr int WS_HH = WS_ST + 32768;
constexpr int WS_QP = WS_HH + 16384;
constexpr int WS_OP = WS_QP + 524288;
constexpr int WS_AP = WS_OP + 524288;
constexpr int WS_FP = WS_AP + 524288;
constexpr int WS_END = WS_FP + 128 * 28 * 128;
constexpr int L_K = 0, L_V = 65536, L_W = 131072, L_M = 147456;
constexpr int L_XLN = L_M, L_OB = L_M, L_QL = L_M + 2048, L_PF = L_M + 3072, L_TQ = L_PF, L_Y = L_PF, L_TB = L_PF + 2048, L_HB = L_M + 2048;
constexpr int L_RMAX = L_M + 11264, L_RSUM = L_M + 11392, L_FLG = L_M + 11552;
constexpr int LDS_BYTES = 163840;
constexpr int AUX_SC1 = 16;
constexpr unsigned SPIN_MAX = 200000u;

#define STAMP(i) do {} while (0)
#define CNT(i, n) do {} while (0)
#define FLUSH() do {} while (0)

DEV float bfu(unsigned hi16) { return __uint_as_float(hi16 << 16); }
DEV float lo_f(unsigned p) { return __uint_as_float(p << 16); }
DEV float hi_f(unsigned p) { return __uint_as_float(p & 0xffff0000u); }
typedef __bf16 v2b __attribute__((ext_vector_type(2)));
typedef float v2f __attribute__((ext_vector_type(2)));
DEV unsigned pk2(float lo, float hi) { return __builtin_bit_cast(unsigned, __builtin_convertvector(v2f{lo, hi}, v2b)); }
DEV float rb(float f) { return lo_f(pk2(f, f)); }
DEV unsigned enc(float v, unsigned t) { return ((__float_as_uint(v) + 0x80u) & 0xffffff00u) | t; }
DEV float dec(unsigned u) { return __uint_as_float(u & 0xffffff00u); }
DEV bool tag_ok(v4u x, unsigned t) { return ((((x.x ^ t) | (x.y ^ t)) | ((x.z ^ t) | (x.w ^ t))) & 0xffu) == 0; }
template <int C> DEV float dpp(float x) { return __builtin_bit_cast(float, __builtin_amdgcn_update_dpp(0, __builtin_bit_cast(int, x), C, 0xf, 0xf, true)); }
DEV float x16_sum(float x) { auto s = __builtin_amdgcn_permlane16_swap(__float_as_uint(x), __float_as_uint(x), false, false); return __uint_as_float(s[0]) + __uint_as_float(s[1]); }
DEV float x32_sum(float x) { auto s = __builtin_amdgcn_permlane32_swap(__float_as_uint(x), __float_as_uint(x), false, false); return __uint_as_float(s[0]) + __uint_as_float(s[1]); }
DEV float x16_max(float x) { auto s = __builtin_amdgcn_permlane16_swap(__float_as_uint(x), __float_as_uint(x), false, false); return fmaxf(__uint_as_float(s[0]), __uint_as_float(s[1])); }
DEV float x32_max(float x) { auto s = __builtin_amdgcn_permlane32_swap(__float_as_uint(x), __float_as_uint(x), false, false); return fmaxf(__uint_as_float(s[0]), __uint_as_float(s[1])); }
DEV float sum8(float x) { x += dpp<0xB1>(x); x += dpp<0x4E>(x); x += dpp<0x141>(x); return x; }
DEV float sum16(float x) { x = sum8(x); x += dpp<0x140>(x); return x; }
DEV float fsqrt(float x) { return __builtin_amdgcn_sqrtf(x); }
DEV float frcp(float x) { return __builtin_amdgcn_rcpf(x); }
#define QDELAY 28
#define EDELAY 20
#define ADELAY 30
#define FDELAY 0
#define HDELAY 0
#define PSLEEP 0
#define MDELAY 0
#define THIN_D 1
#define STR2(x) #x
#define STR(x) STR2(x)
DEV float sum64(float x) { x = sum8(x); x += dpp<0x140>(x); x = x16_sum(x); return x32_sum(x); }
DEV void glds_nt(const void* g, unsigned lds) {
  unsigned keep;
  asm volatile("s_mov_b32 %0, m0\n\ts_mov_b32 m0, %2\n\ts_nop 0\n\tglobal_load_lds_dwordx4 %1, off nt\n\ts_mov_b32 m0, %0" : "=&s"(keep) : "v"(g), "s"(lds) : "memory");
}
DEV void glds(const void* g, unsigned lds) {
  unsigned keep;
  asm volatile("s_mov_b32 %0, m0\n\ts_mov_b32 m0, %2\n\ts_nop 0\n\tglobal_load_lds_dwordx4 %1, off\n\ts_mov_b32 m0, %0" : "=&s"(keep) : "v"(g), "s"(lds) : "memory");
}
#define POLL(N, a, ADDR, CN) do { \
    _Pragma("unroll") for (int i_ = 0; i_ < N; ++i_) { const int i = i_; a[i_] = __builtin_amdgcn_raw_buffer_load_b128(rs, (ADDR), 0, AUX_SC1); } \
    for (unsigned sn = 0; sn < SPIN_MAX; ++sn) { \
      bool ok = true; \
      _Pragma("unroll") for (int i_ = 0; i_ < N; ++i_) ok &= tag_ok(a[i_], tag); \
      CNT(CN, sn + 1); \
      if (__all(ok)) break; \
      if (PSLEEP > 0) __builtin_amdgcn_s_sleep(PSLEEP); \
      FENCE(); \
      _Pragma("unroll") for (int i_ = 0; i_ < N; ++i_) { const int i = i_; a[i_] = __builtin_amdgcn_raw_buffer_load_b128(rs, (ADDR), 0, AUX_SC1); } \
    } \
  } while (0)
#define BAR() asm volatile("s_waitcnt lgkmcnt(0)\n\ts_barrier" ::: "memory")
#define VMCNT(n) asm volatile("s_waitcnt vmcnt(" #n ")" ::: "memory")
#define FENCE() asm volatile("" ::: "memory")
DEV v8b mk(v4u a) { return __builtin_bit_cast(v8b, a); }
DEV v4f mfma(v4u a, v4u b, v4f c) { return __builtin_amdgcn_mfma_f32_16x16x32_bf16(mk(a), mk(b), c, 0, 0, 0); }
DEV v4u trfrag(LAS char* p, int off2) {
  v4s a = __builtin_amdgcn_ds_read_tr16_b64_v4i16((LAS v4s*)p);
  v4s b = __builtin_amdgcn_ds_read_tr16_b64_v4i16((LAS v4s*)(p + off2));
  v2u a2 = __builtin_bit_cast(v2u, a), b2 = __builtin_bit_cast(v2u, b);
  return v4u{a2.x, a2.y, b2.x, b2.y};
}
DEV void lds_wait(LAS unsigned* p, unsigned v) {
  for (unsigned s = 0; s < 50000000u; ++s) { if (*(volatile LAS unsigned*)p >= v) break; }
  FENCE();
}
DEV void lds_inc(LAS unsigned* p, int lane) {
  asm volatile("s_waitcnt lgkmcnt(0)" ::: "memory");
  if (lane == 0) __hip_atomic_fetch_add(p, 1u, __ATOMIC_RELAXED, __HIP_MEMORY_SCOPE_WORKGROUP);
  FENCE();
}
DEV float bperm(int srclane, float v) { return __builtin_bit_cast(float, __builtin_amdgcn_ds_bpermute(srclane << 2, __builtin_bit_cast(int, v))); }
DEV float gelu_t(float z) {
  float u = 0.7978845608028654f * (z + 0.044715f * z * z * z);
  float e = __builtin_amdgcn_exp2f(-2.885390081777927f * u);
  return z * __builtin_amdgcn_rcpf(1.0f + e);
}

__global__ __launch_bounds__(704) void fused_decode_kernel(const u16* __restrict__ x, const u16* __restrict__ kc, const u16* __restrict__ vc,
    const u16* __restrict__ wq, const u16* __restrict__ wo, const u16* __restrict__ w1, const u16* __restrict__ b1, const u16* __restrict__ w2,
    const u16* __restrict__ b2, u16* __restrict__ out, char* ws
    ) {
  __shared__ __attribute__((aligned(1024))) char smem_[LDS_BYTES];
  LAS char* sm = (LAS char*)smem_;
  const unsigned lds0 = (unsigned)(size_t)sm;
  const int lane = threadIdx.x & 63, w = __builtin_amdgcn_readfirstlane(threadIdx.x >> 6);
  const int c = blockIdx.x, xc = c & 7, r = c >> 3;
  const int bq = r >> 2, sp = r & 3;
  const int cl = lane & 15, g = lane >> 4;
  __amdgpu_buffer_rsrc_t rs = __builtin_amdgcn_make_buffer_rsrc(ws, 0, WS_END, 0x00020000);
  LAS unsigned* flg = (LAS unsigned*)(sm + L_FLG);
  STAMP(0);
  const bool isR = r < 16, isM = (r >= 16) && (r < 30);
  const int bpR = r >> 2, cgR = 4 * xc + (r & 3);
  const int im = 14 * xc + (r - 16), hgM = im >> 2, bpM = im & 3;
  const int tq = (lane >> 2) & 3, tp = lane & 3;
  unsigned tag = 0;

  if (w == 8) {
    __builtin_amdgcn_s_setprio(3);
    unsigned E = __builtin_amdgcn_raw_buffer_load_b32(rs, WS_EP, 0, AUX_SC1);
    {
      const int kq = r & 3, cg = r >> 2;
      const char* src = (const char*)wq + (size_t)(128 * kq + (lane >> 3)) * 8192 + (512 * xc + 64 * cg) * 2;
      const int s0 = ((lane & 7) ^ (((lane >> 4) & 1) << 1)) << 4;
#pragma unroll
      for (int j = 0; j < 16; ++j) glds_nt(src + (size_t)j * 8 * 8192 + (s0 ^ ((j & 1) << 6)), lds0 + L_W + j * 1024);
    }
    if (lane < 8) flg[lane] = 0;
    BAR();
    VMCNT(0);
    STAMP(1);
    E = __builtin_amdgcn_readfirstlane(E);
    tag = (E + 1) & 0xff;
    if (lane == 0) flg[4] = tag | 0x100;
    {
      v4u wa[16];
      const int wf = ((tq >> 1) & 1) | ((g & 1) << 1);
#pragma unroll
      for (int t = 0; t < 4; ++t) {
        LAS char* p = sm + L_W + (8 * g + tq) * 128 + tp * 8 + ((t ^ wf) << 5);
#pragma unroll
        for (int st = 0; st < 4; ++st) wa[4 * t + st] = trfrag(p + st * 4096, 512);
      }
      lds_wait(flg + 0, 2);
      v4u xb[4];
#pragma unroll
      for (int st = 0; st < 4; ++st) xb[st] = *(LAS v4u*)(sm + L_XLN + (cl & 7) * 256 + st * 64 + g * 16);
      FENCE();
      v4f acc[4] = {v4f{0, 0, 0, 0}, v4f{0, 0, 0, 0}, v4f{0, 0, 0, 0}, v4f{0, 0, 0, 0}};
#pragma unroll
      for (int st = 0; st < 4; ++st)
#pragma unroll
        for (int t = 0; t < 4; ++t) acc[t] = mfma(wa[4 * t + st], xb[st], acc[t]);
      if (cl < 8) {
#pragma unroll
        for (int t = 0; t < 4; ++t) *(LAS v4f*)(sm + L_TQ + cl * 272 + (16 * t + 4 * g) * 4) = acc[t];
      }
      FENCE();
      const int kq = r & 3, cg = r >> 2;
#pragma unroll
      for (int i = 0; i < 2; ++i) {
        int idx = 64 * i + lane, b = idx >> 4, u = idx & 15;
        v4f v = *(LAS v4f*)(sm + L_TQ + b * 272 + u * 16);
        v4u o = {enc(v.x, tag), enc(v.y, tag), enc(v.z, tag), enc(v.w, tag)};
        __builtin_amdgcn_raw_buffer_store_b128(o, rs, WS_QP + ((xc * 8 + b) * 4 + kq) * 2048 + cg * 256 + u * 16, 0, AUX_SC1);
      }
    }
    STAMP(2);
    {
      v4u q[8];
      const int base = WS_QP + (xc * 8 + bq) * 8192 + lane * 16;
      __builtin_amdgcn_s_sleep(QDELAY);
      POLL(8, q, base + i * 1024, 7);
#pragma unroll
      for (int hf = 0; hf < 2; ++hf) {
        float a0 = (dec(q[hf].x) + dec(q[2 + hf].x)) + (dec(q[4 + hf].x) + dec(q[6 + hf].x));
        float a1 = (dec(q[hf].y) + dec(q[2 + hf].y)) + (dec(q[4 + hf].y) + dec(q[6 + hf].y));
        float a2 = (dec(q[hf].z) + dec(q[2 + hf].z)) + (dec(q[4 + hf].z) + dec(q[6 + hf].z));
        float a3 = (dec(q[hf].w) + dec(q[2 + hf].w)) + (dec(q[4 + hf].w) + dec(q[6 + hf].w));
        *(LAS v2u*)(sm + L_QL + (hf * 256 + 4 * lane) * 2) = v2u{pk2(a0, a1), pk2(a2, a3)};
      }
      lds_inc(flg + 1, lane);
    }
    STAMP(3);
    v2u xres = {0, 0};
    unsigned b2lo = 0, b2hi = 0;
    if (isR) {
      xres = *(const v2u*)(x + (2 * bpR + ((lane >> 2) & 1)) * 512 + 16 * cgR + 4 * (lane & 3));
      v2u bb = *(const v2u*)(b2 + 16 * cgR + 4 * (lane & 3));
      b2lo = bb.x; b2hi = bb.y;
    }
    BAR();
    BAR();
    {
      const int h = lane & 3;
      v4f m0 = *(LAS v4f*)(sm + L_RMAX + h * 32), m1 = *(LAS v4f*)(sm + L_RMAX + h * 32 + 16);
      v4f s0 = *(LAS v4f*)(sm + L_RSUM + h * 32), s1 = *(LAS v4f*)(sm + L_RSUM + h * 32 + 16);
      float m = fmaxf(fmaxf(fmaxf(m0.x, m0.y), fmaxf(m0.z, m0.w)), fmaxf(fmaxf(m1.x, m1.y), fmaxf(m1.z, m1.w)));
      float l = ((s0.x + s0.y) + (s0.z + s0.w)) + ((s1.x + s1.y) + (s1.z + s1.w));
      float v = (lane & 4) ? l : m;
      v4u o = {enc(dpp<0x00>(v), tag), enc(dpp<0x55>(v), tag), enc(dpp<0xAA>(v), tag), enc(dpp<0xFF>(v), tag)};
      if (lane == 0 || lane == 4) __builtin_amdgcn_raw_buffer_store_b128(o, rs, WS_ST + (xc * 8 + bq) * 512 + sp * 128 + (lane >> 2) * 16, 0, AUX_SC1);
    }
    STAMP(4);
    BAR();
    if (isM) { BAR(); BAR(); BAR(); BAR(); FLUSH(); return; }
    if (!isR) { FLUSH(); return; }
    BAR();
    float h0, h1, h2, h3;
    {
      v4u a[4];
      const int base = WS_AP + (bpR * 32 + cgR) * 4096 + lane * 16;
      if (ADELAY > 0) __builtin_amdgcn_s_sleep(ADELAY);
      POLL(4, a, base + i * 1024, 5);
      STAMP(9);
      float s[4] = {(dec(a[0].x) + dec(a[1].x)) + (dec(a[2].x) + dec(a[3].x)), (dec(a[0].y) + dec(a[1].y)) + (dec(a[2].y) + dec(a[3].y)),
                    (dec(a[0].z) + dec(a[1].z)) + (dec(a[2].z) + dec(a[3].z)), (dec(a[0].w) + dec(a[1].w)) + (dec(a[2].w) + dec(a[3].w))};
#pragma unroll
      for (int i = 0; i < 4; ++i) { s[i] += dpp<0x128>(s[i]); s[i] = x16_sum(s[i]); s[i] = x32_sum(s[i]); }
      h0 = rb(lo_f(xres.x) + rb(s[0])); h1 = rb(hi_f(xres.x) + rb(s[1])); h2 = rb(lo_f(xres.y) + rb(s[2])); h3 = rb(hi_f(xres.y) + rb(s[3]));
      v4u o = {__float_as_uint(h0) | tag, __float_as_uint(h1) | tag, __float_as_uint(h2) | tag, __float_as_uint(h3) | tag};
      if (lane < 8) __builtin_amdgcn_raw_buffer_store_b128(o, rs, WS_HH + (bpR * 32 + cgR) * 128 + lane * 16, 0, AUX_SC1);
      if (c == 0 && lane == 0) __builtin_amdgcn_raw_buffer_store_b32(E + 1, rs, WS_EP, 0, AUX_SC1);
    }
    STAMP(10);
    {
      v4u a[4];
      const int base = WS_FP + (bpR * 32 + cgR) * (28 * 128);
      const int o3 = (lane < 32 ? 192 + lane : 128 + lane) * 16;
      if (FDELAY > 0) __builtin_amdgcn_s_sleep(FDELAY);
      POLL(4, a, base + (i < 3 ? i * 1024 + lane * 16 : o3), 6);
      STAMP(11);
      const float k3 = lane < 32 ? 1.0f : 0.0f;
      float s[4] = {(dec(a[0].x) + dec(a[1].x)) + (dec(a[2].x) + k3 * dec(a[3].x)), (dec(a[0].y) + dec(a[1].y)) + (dec(a[2].y) + k3 * dec(a[3].y)),
                    (dec(a[0].z) + dec(a[1].z)) + (dec(a[2].z) + k3 * dec(a[3].z)), (dec(a[0].w) + dec(a[1].w)) + (dec(a[2].w) + k3 * dec(a[3].w))};
#pragma unroll
      for (int i = 0; i < 4; ++i) { s[i] += dpp<0x128>(s[i]); s[i] = x16_sum(s[i]); s[i] = x32_sum(s[i]); }
      float o0 = h0 + rb(rb(s[0]) + lo_f(b2lo)), o1 = h1 + rb(rb(s[1]) + hi_f(b2lo)), o2 = h2 + rb(rb(s[2]) + lo_f(b2hi)), o3f = h3 + rb(rb(s[3]) + hi_f(b2hi));
      __amdgpu_buffer_rsrc_t ro = __builtin_amdgcn_make_buffer_rsrc(out, 0, 8192, 0x00020000);
      if (lane < 8) __builtin_amdgcn_raw_buffer_store_b64(v2u{pk2(o0, o1), pk2(o2, o3f)}, ro, ((2 * bpR + (lane >> 2)) * 512 + 16 * cgR + 4 * (lane & 3)) * 2, 0, AUX_SC1);
    }
    STAMP(12);
    FLUSH();
    return;
  }
  if (w >= 9) {
    __builtin_amdgcn_s_setprio(2);
    v4u xr[4], xs;
    const int xb_ = 4 * (w - 9) + g;
    const u16* xp = x + xb_ * 512 + cl * 8;
#pragma unroll
    for (int i = 0; i < 4; ++i) xr[i] = *(const v4u*)(xp + 128 * i);
    xs = *(const v4u*)(xp + 128 * (r & 3));
    BAR();
    {
      v2f f[16], s2[4] = {v2f{0, 0}, v2f{0, 0}, v2f{0, 0}, v2f{0, 0}};
#pragma unroll
      for (int i = 0; i < 16; ++i) { f[i] = v2f{lo_f(xr[i >> 2][i & 3]), hi_f(xr[i >> 2][i & 3])}; s2[i & 3] += f[i]; }
      v2f st = (s2[0] + s2[1]) + (s2[2] + s2[3]);
      float mean = sum16(st.x + st.y) * (1.0f / 512.0f);
      v2f q2[4] = {v2f{0, 0}, v2f{0, 0}, v2f{0, 0}, v2f{0, 0}}, mm = {mean, mean};
#pragma unroll
      for (int i = 0; i < 16; ++i) { v2f d = f[i] - mm; q2[i & 3] = __builtin_elementwise_fma(d, d, q2[i & 3]); }
      v2f qt = (q2[0] + q2[1]) + (q2[2] + q2[3]);
      float var = sum16(qt.x + qt.y) * (1.0f / 512.0f);
      float mb = rb(mean), inv = frcp(rb(fsqrt(rb(rb(var) + 1e-5f))));
      v4u o;
#pragma unroll
      for (int j = 0; j < 4; ++j) o[j] = pk2(rb(lo_f(xs[j]) - mb) * inv, rb(hi_f(xs[j]) - mb) * inv);
      *(LAS v4u*)(sm + L_XLN + xb_ * 256 + cl * 16) = o;
      lds_inc(flg + 0, lane);
    }
    STAMP(1);
    FLUSH();
    return;
  }
  v2u b1v = {0, 0};
  if (isM && w < 4) b1v = *(const v2u*)(b1 + 64 * hgM + 16 * w + 4 * g);
  BAR();
  __builtin_amdgcn_s_setprio(1);
  v4u wr[4];
  {
    bool full = false;
    const char* kb = (const char*)kc + ((size_t)(bq * 8 + xc) * 4096 + 3072 + sp * 256 + 32 * w) * 256;
#pragma unroll
    for (int i = 0; i < 8; ++i) {
      int key = 4 * i + g;
      glds_nt(kb + key * 256 + (((lane & 15) ^ (key & 15)) << 4), lds0 + L_K + (32 * w + 4 * i) * 256);
      if (THIN_D > 0 && i >= THIN_D && !full) { asm volatile("s_waitcnt vmcnt(" STR(THIN_D) ")" ::: "memory"); full = __builtin_amdgcn_readfirstlane(*(volatile LAS unsigned*)(flg + 1)) != 0; }
    }
    __builtin_amdgcn_s_setprio(0);
    const char* vb = (const char*)vc + ((size_t)(bq * 8 + xc) * 4096 + 3072 + sp * 256 + 32 * w) * 256;
    const int pos = lane & 15;
    const int o0 = g * 256 + ((((pos >> 1) ^ g) << 5) | ((pos & 1) << 4));
    const int o1 = g * 256 + ((((pos >> 1) ^ (4 + g)) << 5) | ((pos & 1) << 4));
#pragma unroll
    for (int j = 0; j < 8; ++j) {
      glds_nt(vb + j * 1024 + ((j & 1) ? o1 : o0), lds0 + L_V + (8 * w + j) * 1024);
      if (THIN_D > 0 && j < 7 && !full) { asm volatile("s_waitcnt vmcnt(" STR(THIN_D) ")" ::: "memory"); full = __builtin_amdgcn_readfirstlane(*(volatile LAS unsigned*)(flg + 1)) != 0; }
    }
  }
  VMCNT(8);
  STAMP(1);
  lds_wait(flg + 1, 1);
  STAMP(2);
  __builtin_amdgcn_s_setprio(2);
  {
    v4f acc[2] = {v4f{0, 0, 0, 0}, v4f{0, 0, 0, 0}};
    v4u qf[4], kf[8];
#pragma unroll
    for (int st = 0; st < 4; ++st) {
      qf[st] = *(LAS v4u*)(sm + L_QL + (cl & 3) * 256 + st * 64 + g * 16);
#pragma unroll
      for (int tt = 0; tt < 2; ++tt) kf[2 * st + tt] = *(LAS v4u*)(sm + L_K + (32 * w + 16 * tt + cl) * 256 + (((4 * st + g) ^ cl) << 4));
    }
    FENCE();
#pragma unroll
    for (int st = 0; st < 4; ++st)
#pragma unroll
      for (int tt = 0; tt < 2; ++tt) acc[tt] = mfma(kf[2 * st + tt], qf[st], acc[tt]);
    float s[8];
#pragma unroll
    for (int i = 0; i < 8; ++i) s[i] = rb(rb(acc[i >> 2][i & 3]) * 0.08838834764831845f);
    float mx = fmaxf(fmaxf(fmaxf(s[0], s[1]), fmaxf(s[2], s[3])), fmaxf(fmaxf(s[4], s[5]), fmaxf(s[6], s[7])));
    mx = x32_max(x16_max(mx));
    if (lane < 4) *(LAS float*)(sm + L_RMAX + lane * 32 + w * 4) = mx;
    BAR();
    v4f m0 = *(LAS v4f*)(sm + L_RMAX + (cl & 3) * 32), m1 = *(LAS v4f*)(sm + L_RMAX + (cl & 3) * 32 + 16);
    float m = fmaxf(fmaxf(fmaxf(m0.x, m0.y), fmaxf(m0.z, m0.w)), fmaxf(fmaxf(m1.x, m1.y), fmaxf(m1.z, m1.w)));
    float ls = 0;
#pragma unroll
    for (int i = 0; i < 8; ++i) { s[i] = __builtin_amdgcn_exp2f((s[i] - m) * 1.4426950408889634f); ls += s[i]; }
    ls = x32_sum(x16_sum(ls));
    if (lane < 4) *(LAS float*)(sm + L_RSUM + lane * 32 + w * 4) = ls;
    *(LAS v4u*)(sm + L_PF + w * 1024 + lane * 16) = v4u{pk2(s[0], s[1]), pk2(s[2], s[3]), pk2(s[4], s[5]), pk2(s[6], s[7])};
  }
  BAR();
  STAMP(3);
  tag = flg[4] & 0xff;
  v4u pf[8];
#pragma unroll
  for (int m = 0; m < 8; ++m) pf[m] = *(LAS v4u*)(sm + L_PF + m * 1024 + lane * 16);
  if (isR) {
    const char* src = (const char*)wo + (size_t)(512 * xc + 128 * g + 32 * (r >> 2) + 8 * (w >> 1) + 4 * (w & 1)) * 1024 + (r & 3) * 256;
    const int pos = lane & 15;
#pragma unroll
    for (int jj = 0; jj < 4; ++jj) {
      int f8 = g | ((jj >> 1) << 2);
      const char* ap = src + (size_t)(2 * (jj >> 1) + (jj & 1)) * 1024 + ((((pos >> 1) ^ f8) << 5) | ((pos & 1) << 4));
      asm volatile("global_load_dwordx4 %0, %1, off nt" : "=v"(wr[jj]) : "v"(ap) : "memory");
    }
    VMCNT(4);
  } else {
    VMCNT(0);
  }
  STAMP(4);
  BAR();
  {
    LAS char* p = sm + L_V + (4 * g + tq) * 256 + ((w ^ (4 * (g & 1) + tq)) << 5) + tp * 8;
    v4u vf[8];
#pragma unroll
    for (int m = 0; m < 8; ++m) vf[m] = trfrag(p + m * 8192, 4096);
    FENCE();
    v4f acc = {0, 0, 0, 0}, acc1 = {0, 0, 0, 0};
#pragma unroll
    for (int m = 0; m < 8; m += 2) { acc = mfma(pf[m], vf[m], acc); acc1 = mfma(pf[m + 1], vf[m + 1], acc1); }
    acc += acc1;
    v4u o = {enc(acc.x, tag), enc(acc.y, tag), enc(acc.z, tag), enc(acc.w, tag)};
    if (lane < 16)
      __builtin_amdgcn_raw_buffer_store_b128(o, rs, WS_OP + (((xc * 4 + (w >> 1)) * 8 + bq) * 4 + sp) * 512 + ((w & 1) * 16 + lane) * 16, 0, AUX_SC1);
  }
  STAMP(6);
  const int f8 = (tq & 3) | ((g & 1) << 2);
  if (isR) {
    {
      v4u a[3];
      const int base = WS_OP + ((xc * 4 + (r >> 2)) * 8 + w) * 2048 + lane * 16;
      const int sb = WS_ST + (xc * 8 + w) * 512 + (lane & 3) * 128 + (lane & 4) * 4;
      float fa[4], fb[4];
      __builtin_amdgcn_s_sleep(EDELAY);
      a[2] = __builtin_amdgcn_raw_buffer_load_b128(rs, sb, 0, AUX_SC1);
      a[0] = __builtin_amdgcn_raw_buffer_load_b128(rs, base, 0, AUX_SC1);
      a[1] = __builtin_amdgcn_raw_buffer_load_b128(rs, base + 1024, 0, AUX_SC1);
      for (unsigned sn = 0; sn < SPIN_MAX; ++sn) {
        if (__all(tag_ok(a[2], tag))) break;
        FENCE();
        a[2] = __builtin_amdgcn_raw_buffer_load_b128(rs, sb, 0, AUX_SC1);
      }
      asm volatile("s_waitcnt vmcnt(0)" : "+v"(wr[0]), "+v"(wr[1]), "+v"(wr[2]), "+v"(wr[3]) : "v"(a[2].x) : "memory");
#pragma unroll
      for (int jj = 0; jj < 4; ++jj) *(LAS v4u*)(sm + L_K + (4 * w + jj) * 1024 + lane * 16) = wr[jj];
#pragma unroll
      for (int i = 0; i < 4; ++i) {
        float own = dec(a[2][i]), oth = dpp<0x124>(own);
        float mi = (lane & 4) ? oth : own, li = (lane & 4) ? own : oth;
        float M = fmaxf(mi, dpp<0xB1>(mi)); M = fmaxf(M, dpp<0x4E>(M));
        float sc = __builtin_amdgcn_exp2f((mi - M) * 1.4426950408889634f);
        float L = li * sc; L += dpp<0xB1>(L); L += dpp<0x4E>(L);
        float fi = sc * frcp(L);
        float f0 = dpp<0x00>(fi), f1 = dpp<0x55>(fi), f2 = dpp<0xAA>(fi), f3 = dpp<0xFF>(fi);
        fa[i] = lane < 32 ? f0 : f1; fb[i] = lane < 32 ? f2 : f3;
      }
      for (unsigned sn = 0; sn < SPIN_MAX; ++sn) {
        CNT(5, sn + 1);
        if (sn == 0) STAMP(9);
        if (__all(tag_ok(a[0], tag) && tag_ok(a[1], tag))) break;
        FENCE();
        a[0] = __builtin_amdgcn_raw_buffer_load_b128(rs, base, 0, AUX_SC1);
        a[1] = __builtin_amdgcn_raw_buffer_load_b128(rs, base + 1024, 0, AUX_SC1);
      }
      STAMP(7);
      float ov[4];
#pragma unroll
      for (int i = 0; i < 4; ++i) ov[i] = x32_sum(dec(a[0][i]) * fa[i] + dec(a[1][i]) * fb[i]);
      if (lane < 32) *(LAS v2u*)(sm + L_OB + (lane >> 3) * 512 + ((lane & 7) >> 1) * 128 + w * 16 + (lane & 1) * 8) = v2u{pk2(ov[0], ov[1]), pk2(ov[2], ov[3])};
    }
    BAR();
    {
      v4u ob[4], wof[4];
      LAS char* p = sm + L_K + (8 * g + tq) * 256 + ((w ^ f8) << 5) + tp * 8;
#pragma unroll
      for (int m = 0; m < 4; ++m) { ob[m] = *(LAS v4u*)(sm + L_OB + m * 512 + g * 128 + (cl & 7) * 16); wof[m] = trfrag(p + m * 8192, 1024); }
      FENCE();
      v4f acc = {0, 0, 0, 0}, acc1 = {0, 0, 0, 0};
      acc = mfma(wof[0], ob[0], acc); acc1 = mfma(wof[1], ob[1], acc1);
      acc = mfma(wof[2], ob[2], acc); acc1 = mfma(wof[3], ob[3], acc1);
      acc += acc1;
      const int src = 2 * (lane >> 3) + ((lane >> 2) & 1) + 16 * (lane & 3);
      v4u o = {enc(bperm(src, acc.x), tag), enc(bperm(src, acc.y), tag), enc(bperm(src, acc.z), tag), enc(bperm(src, acc.w), tag)};
      if (lane < 32)
        __builtin_amdgcn_raw_buffer_store_b128(o, rs, WS_AP + (((lane >> 3) * 32 + 8 * (r & 3) + w) * 32 + (xc * 4 + (r >> 2))) * 128 + (lane & 7) * 16, 0, AUX_SC1);
    }
    STAMP(8);
  } else if (isM) {
    if (MDELAY > 0) __builtin_amdgcn_s_sleep(MDELAY);
    {
      const char* src = (const char*)w1 + (size_t)(64 * w + (lane >> 3)) * 3584 + hgM * 128;
      const int s0 = ((lane & 7) ^ (((lane >> 4) & 1) << 1)) << 4;
#pragma unroll
      for (int j = 0; j < 8; ++j) glds(src + (size_t)j * 8 * 3584 + (s0 ^ ((j & 1) << 6)), lds0 + L_K + (8 * w + j) * 1024);
    }
    BAR();
    {
      const char* src = (const char*)w2 + (size_t)(64 * hgM + 32 * (w >> 2) + 4 * (w & 3)) * 1024;
#pragma unroll
      for (int j = 0; j < 8; ++j) {
        int ff = (j & 3) | ((w & 1) << 2);
        glds(src + (size_t)(16 * (j >> 2) + (j & 3)) * 1024 + ((((lane >> 1) ^ ff) << 5) | ((lane & 1) << 4)), lds0 + L_V + (8 * w + j) * 1024);
      }
    }
    VMCNT(0);
    STAMP(9);
    BAR();
    v4u bf[8];
    LAS char* pw2 = sm + L_V + (8 * g + tq) * 1024 + tp * 8;
    if (w < 4) {
      v4u af[16];
      const int wf = ((tq >> 1) & 1) | ((g & 1) << 1);
      LAS char* p = sm + L_K + (8 * g + tq) * 128 + tp * 8 + ((w ^ wf) << 5);
#pragma unroll
      for (int st = 0; st < 16; ++st) af[st] = trfrag(p + st * 4096, 512);
      BAR();
      STAMP(11);
      v4u yb[16];
#pragma unroll
      for (int st = 0; st < 16; ++st) yb[st] = *(LAS v4u*)(sm + L_Y + (cl & 1) * 1024 + st * 64 + g * 16);
      FENCE();
      v4f acc = {0, 0, 0, 0}, acc1 = {0, 0, 0, 0};
#pragma unroll
      for (int st = 0; st < 16; st += 2) { acc = mfma(af[st], yb[st], acc); acc1 = mfma(af[st + 1], yb[st + 1], acc1); }
      acc += acc1;
      float h0 = gelu_t(rb(rb(acc.x) + lo_f(b1v.x))), h1 = gelu_t(rb(rb(acc.y) + hi_f(b1v.x)));
      float h2 = gelu_t(rb(rb(acc.z) + lo_f(b1v.y))), h3 = gelu_t(rb(rb(acc.w) + hi_f(b1v.y)));
      if (cl < 2) *(LAS v2u*)(sm + L_HB + (w >> 1) * 128 + g * 32 + cl * 16 + (w & 1) * 8) = v2u{pk2(h0, h1), pk2(h2, h3)};
      FENCE();
#pragma unroll
      for (int j = 0; j < 4; ++j)
#pragma unroll
        for (int m = 0; m < 2; ++m) bf[2 * j + m] = trfrag(pw2 + (((4 * w + j) ^ f8) << 5) + m * 32768, 4096);
      asm volatile("s_waitcnt lgkmcnt(15)\n\ts_barrier" ::: "memory");
    } else {
#pragma unroll
      for (int j = 0; j < 4; ++j)
#pragma unroll
        for (int m = 0; m < 2; ++m) bf[2 * j + m] = trfrag(pw2 + (((4 * w + j) ^ f8) << 5) + m * 32768, 4096);
      if (w < 6) {
        const int bl = w - 4;
        v4u a[2];
        const int base = WS_HH + bpM * 4096 + (lane >> 2) * 128 + bl * 64 + (lane & 3) * 16;
        if (HDELAY > 0) __builtin_amdgcn_s_sleep(HDELAY);
        POLL(2, a, base + i * 2048, 5);
        STAMP(10);
        v2f v[4] = {v2f{dec(a[0].x), dec(a[0].y)}, v2f{dec(a[0].z), dec(a[0].w)}, v2f{dec(a[1].x), dec(a[1].y)}, v2f{dec(a[1].z), dec(a[1].w)}};
        unsigned pk[4];
#pragma unroll
        for (int pass = 0; pass < 2; ++pass) {
          const float x0 = __builtin_bit_cast(float, __builtin_amdgcn_readfirstlane(__builtin_bit_cast(int, v[0].x)));
          v2f d[4];
#pragma unroll
          for (int i = 0; i < 4; ++i) d[i] = v[i] - v2f{x0, x0};
          v2f s2 = (d[0] + d[1]) + (d[2] + d[3]);
          v2f q2 = __builtin_elementwise_fma(d[2], d[2], d[0] * d[0]) + __builtin_elementwise_fma(d[3], d[3], d[1] * d[1]);
          float s = s2.x + s2.y, q = q2.x + q2.y;
          s += dpp<0xB1>(s); q += dpp<0xB1>(q); s += dpp<0x4E>(s); q += dpp<0x4E>(q); s += dpp<0x141>(s); q += dpp<0x141>(q);
          s += dpp<0x140>(s); q += dpp<0x140>(q); s = x16_sum(s); q = x16_sum(q); s = x32_sum(s); q = x32_sum(q);
          float md = s * (1.0f / 512.0f);
          float var = fmaxf(q * (1.0f / 512.0f) - md * md, 0.0f);
          float mb = rb(x0 + md), inv = frcp(rb(fsqrt(rb(rb(var) + 1e-5f))));
#pragma unroll
          for (int i = 0; i < 4; ++i) {
            v2f t = v[i] - v2f{mb, mb};
            unsigned p = pk2(t.x, t.y);
            t = v2f{lo_f(p), hi_f(p)} * v2f{inv, inv};
            pk[i] = pk2(t.x, t.y);
            v[i] = v2f{lo_f(pk[i]), hi_f(pk[i])};
          }
        }
        *(LAS v2u*)(sm + L_Y + bl * 1024 + lane * 8) = v2u{pk[0], pk[1]};
        *(LAS v2u*)(sm + L_Y + bl * 1024 + 512 + lane * 8) = v2u{pk[2], pk[3]};
      }
      BAR();
      BAR();
    }
    {
      v4u hb0 = *(LAS v4u*)(sm + L_HB + g * 32 + (cl & 1) * 16), hb1 = *(LAS v4u*)(sm + L_HB + 128 + g * 32 + (cl & 1) * 16);
      v4f acc[4];
#pragma unroll
      for (int j = 0; j < 4; ++j) { acc[j] = mfma(bf[2 * j], hb0, v4f{0, 0, 0, 0}); acc[j] = mfma(bf[2 * j + 1], hb1, acc[j]); }
      if (cl < 2) {
#pragma unroll
        for (int j = 0; j < 4; ++j) *(LAS v4f*)(sm + L_TB + w * 512 + j * 128 + cl * 64 + g * 16) = acc[j];
      }
      FENCE();
      v4f v = *(LAS v4f*)(sm + L_TB + w * 512 + (lane & 31) * 16);
      v4u o = {enc(v.x, tag), enc(v.y, tag), enc(v.z, tag), enc(v.w, tag)};
      if (lane < 32)
        __builtin_amdgcn_raw_buffer_store_b128(o, rs, WS_FP + ((bpM * 32 + 4 * w + (lane >> 3)) * 28 + hgM) * 128 + (lane & 7) * 16, 0, AUX_SC1);
    }
    STAMP(12);
  }
  FLUSH();
}

extern "C" void kernel_launch(void* const* d_in, const int* in_sizes, int n_in, void* d_out, int out_size, void* d_ws, size_t ws_size, hipStream_t stream) {
  fused_decode_kernel<<<256, 704, 0, stream>>>((const u16*)d_in[0], (const u16*)d_in[1], (const u16*)d_in[2], (const u16*)d_in[3], (const u16*)d_in[4],
                                               (const u16*)d_in[6], (const u16*)d_in[7], (const u16*)d_in[8], (const u16*)d_in[9], (u16*)d_out, (char*)d_ws);
}
```
